```python
import torch

def _softmax(x, axis=-1):
    return torch.softmax(x, dim=axis)

def _gelu(x):
    return torch.nn.functional.gelu(x, approximate='tanh')

def _silu(x):
    return torch.nn.functional.silu(x)

def _sigmoid(x):
    return torch.sigmoid(x)

def _layernorm(x, axis=-1, eps=1e-05):
    m = x.mean(dim=axis, keepdim=True)
    v = x.var(dim=axis, keepdim=True, unbiased=False)
    return (x - m) / torch.sqrt(v + eps)

def _scatter_add(tgt, idx, src, axis=0):
    return tgt.clone().index_add_(axis, idx, src)

def _segment_sum(src, seg, n):
    out = torch.zeros((n,) + src.shape[1:], dtype=src.dtype)
    return out.index_add_(0, seg, src)

def _pad(x, pw):
    flat = tuple((v for pair in reversed(pw) for v in pair))
    return torch.nn.functional.pad(x, flat)
kernel_name = 'adaLN_cond__gqa_block_cached__glu_gated_bilinear__decode'

def setup_inputs():

    _w = lambda a: (lambda t: t.to(torch.bfloat16) if t.is_floating_point() else t)(torch.as_tensor(a))

    def _A_setup():
        B, Sq, D = (1, 1, 640)
        Hq, Hkv, Sk, Dh = (32, 32, 4096, 192)
        assert Hq % Hkv == 0, (Hq, Hkv)
        return {'x': _w((lambda *s: torch.randn(*s, dtype=torch.bfloat16) * 0.1)(B, Sq, D)), 'k_cache': _w((lambda *s: torch.randn(*s, dtype=torch.bfloat16) * 0.8)(B, Hkv, Sk, Dh)), 'v_cache': _w((lambda *s: torch.randn(*s, dtype=torch.bfloat16) * 3.2)(B, Hkv, Sk, Dh)), 'wq': _w((lambda *s: torch.randn(*s, dtype=torch.bfloat16) * 0.8)(D, Hq * Dh) * D ** (-0.5)), 'wo': _w((lambda *s: torch.randn(*s, dtype=torch.bfloat16) * 3.2)(Hq * Dh, D) * (Hq * Dh) ** (-0.5))}
    _A_in = _A_setup()

    def _B_setup():
        B, S, D, F = (1, 1, 640, 1536)
        return {'x': _w((lambda *s: torch.randn(*s, dtype=torch.bfloat16) * 0.1)(B, S, D)), 'w_gate': _w((lambda *s: torch.randn(*s, dtype=torch.bfloat16) * 3.2)(D, F) * D ** (-0.5)), 'w_up': _w((lambda *s: torch.randn(*s, dtype=torch.bfloat16) * 3.2)(D, F) * D ** (-0.5)), 'w_down': _w((lambda *s: torch.randn(*s, dtype=torch.bfloat16) * 3.2)(F, D) * F ** (-0.5))}
    _B_in = _B_setup()
    return {'A_x': _A_in['x'], 'A_k_cache': _A_in['k_cache'], 'A_v_cache': _A_in['v_cache'], 'A_wq': _A_in['wq'], 'A_wo': _A_in['wo'], 'B_x': _B_in['x'], 'B_w_gate': _B_in['w_gate'], 'B_w_up': _B_in['w_up'], 'B_w_down': _B_in['w_down'], 'c': _w((lambda *s: torch.randn(*s, dtype=torch.bfloat16) * 0.4)(1, 640) * 8.0), 'w_mod': _w((lambda *s: torch.randn(*s, dtype=torch.bfloat16) * 0.4)(640, 6 * 640) * 640 ** (-0.5) * 8.0)}

def reference(A_x, A_k_cache, A_v_cache, A_wq, A_wo, B_x, B_w_gate, B_w_up, B_w_down, c, w_mod):

    def _A_ref(x, k_cache, v_cache, wq, wo):
        B, Sq, D = x.shape
        Hq, Dh = (32, 192)
        Hkv = k_cache.shape[1]
        rep = Hq // Hkv
        q = (lambda x, *d: x.permute(*d))((x @ wq).reshape(B, Sq, Hq, Dh), 0, 2, 1, 3)
        k = (lambda x, n, axis: torch.repeat_interleave(x, n, dim=axis))(k_cache, rep, axis=1)
        v = (lambda x, n, axis: torch.repeat_interleave(x, n, dim=axis))(v_cache, rep, axis=1)
        scores = torch.einsum('bhqd,bhkd->bhqk', q, k) * Dh ** (-0.5)
        p = _softmax(scores, axis=-1)
        o = torch.einsum('bhqk,bhkd->bhqd', p, v)
        return (lambda x, *d: x.permute(*d))(o, 0, 2, 1, 3).reshape(B, Sq, Hq * Dh) @ wo

    def _B_ref(x, w_gate, w_up, w_down):
        g = torch.einsum('bsd,df->bsf', x, w_gate)
        u = torch.einsum('bsd,df->bsf', x, w_up)
        h = g * u
        return torch.einsum('bsf,fd->bsd', h, w_down)
    x = A_x
    mod = (_silu(c) @ w_mod).reshape(c.shape[0], 6, 1, -1)
    sa, ca, ga, sb, cb, gb = (mod[:, 0].clone(), mod[:, 1].clone(), mod[:, 2].clone(), mod[:, 3].clone(), mod[:, 4].clone(), mod[:, 5].clone())
    h = _layernorm(x, axis=-1) * (1 + ca) + sa
    x = x + ga * _A_ref(h, A_k_cache, A_v_cache, A_wq, A_wo)
    h = _layernorm(x, axis=-1) * (1 + cb) + sb
    x = x + gb * _B_ref(h, B_w_gate, B_w_up, B_w_down)
    return x
```

```cpp
#include <hip/hip_runtime.h>
#include <hip/hip_bf16.h>
#include <stdint.h>

#define MIMIC 1

typedef unsigned short u16;
constexpr int D = 640, H = 32, SK = 4096, DH = 192, F = 1536, QD = H * DH;
constexpr int NSPLIT = 16, KPS = SK / NSPLIT;

__device__ __forceinline__ float bf2f(u16 b) { return __uint_as_float((unsigned)b << 16); }
__device__ __forceinline__ u16 f2bf(float f) { unsigned u = __float_as_uint(f); u += 0x7FFFu + ((u >> 16) & 1u); return (u16)(u >> 16); }
__device__ __forceinline__ float rb(float f) {
  return bf2f(f2bf(f));
}

__device__ float block_sum(float v, float* red) {
  for (int o = 32; o > 0; o >>= 1) v += __shfl_xor(v, o);
  int w = threadIdx.x >> 6, nw = blockDim.x >> 6;
  __syncthreads();
  if ((threadIdx.x & 63) == 0) red[w] = v;
  __syncthreads();
  float s = 0.f;
  for (int i = 0; i < nw; ++i) s += red[i];
  return s;
}
__device__ float block_max(float v, float* red) {
  for (int o = 32; o > 0; o >>= 1) v = fmaxf(v, __shfl_xor(v, o));
  int w = threadIdx.x >> 6, nw = blockDim.x >> 6;
  __syncthreads();
  if ((threadIdx.x & 63) == 0) red[w] = v;
  __syncthreads();
  float s = -INFINITY;
  for (int i = 0; i < nw; ++i) s = fmaxf(s, red[i]);
  return s;
}

__device__ void gemv64(const float* xs, const u16* W, int K, int N, int n0, float* red, float& outv, bool& valid) {
  int col = n0 + (threadIdx.x & 63), kg = threadIdx.x >> 6;
  float acc = 0.f;
  for (int k = kg; k < K; k += 4) acc += xs[k] * bf2f(W[(size_t)k * N + col]);
  __syncthreads();
  red[threadIdx.x] = acc;
  __syncthreads();
  valid = threadIdx.x < 64;
  outv = 0.f;
  if (valid) outv = red[threadIdx.x] + red[threadIdx.x + 64] + red[threadIdx.x + 128] + red[threadIdx.x + 192];
}

__device__ void ada_ln(const float* xs, const float* sc, const float* sh, float* hs, float* red) {
  float s = 0.f;
  for (int i = threadIdx.x; i < D; i += blockDim.x) s += xs[i];
  float mean_f = block_sum(s, red) / D;
  float m = rb(mean_f);
  float v = 0.f;
  for (int i = threadIdx.x; i < D; i += blockDim.x) { float d = xs[i] - mean_f; v += d * d; }
  float var = rb(block_sum(v, red) / D);
  float den = rb(sqrtf(rb(var + 1e-5f)));
  for (int i = threadIdx.x; i < D; i += blockDim.x) {
    float ln = rb(rb(xs[i] - m) / den);
    hs[i] = rb(rb(ln * rb(1.f + sc[i])) + sh[i]);
  }
  __syncthreads();
}

__global__ void k_mod(const u16* c, const u16* wmod, float* mod) {
  __shared__ float xs[D]; __shared__ float red[256];
  for (int i = threadIdx.x; i < D; i += 256) { float v = bf2f(c[i]); xs[i] = rb(v / (1.f + __expf(-v))); }
  __syncthreads();
  float o; bool ok; gemv64(xs, wmod, D, 6 * D, blockIdx.x * 64, red, o, ok);
  if (ok) mod[blockIdx.x * 64 + threadIdx.x] = rb(o);
}
__global__ void k_q(const u16* x, const float* mod, const u16* wq, float* q) {
  __shared__ float xs[D]; __shared__ float hs[D]; __shared__ float red[256];
  for (int i = threadIdx.x; i < D; i += 256) xs[i] = bf2f(x[i]);
  __syncthreads();
  ada_ln(xs, mod + D, mod, hs, red);
  float o; bool ok; gemv64(hs, wq, D, QD, blockIdx.x * 64, red, o, ok);
  if (ok) q[blockIdx.x * 64 + threadIdx.x] = rb(o);
}
__global__ void k_scores(const float* q, const u16* K, float* sc) {
  int h = blockIdx.y, k = blockIdx.x * 256 + threadIdx.x;
  __shared__ float qs[DH];
  for (int i = threadIdx.x; i < DH; i += 256) qs[i] = q[h * DH + i];
  __syncthreads();
  const u16* kr = K + ((size_t)h * SK + k) * DH;
  float acc = 0.f;
  for (int d = 0; d < DH; ++d) acc += qs[d] * bf2f(kr[d]);
  const float scale = rb(0.07216878364870322f);
  (void)scale;
  sc[h * SK + k] = rb(rb(acc) * 0.07216878364870322f);
}
__global__ void k_pv(const float* sc, const u16* V, float* o) {
  int h = blockIdx.x;
  __shared__ float ps[SK]; __shared__ float red[256];
  float m = -INFINITY;
  for (int i = threadIdx.x; i < SK; i += 256) m = fmaxf(m, sc[h * SK + i]);
  m = block_max(m, red);
  float s = 0.f;
  for (int i = threadIdx.x; i < SK; i += 256) { float e = __expf(sc[h * SK + i] - m); ps[i] = e; s += e; }
  s = block_sum(s, red);
  for (int i = threadIdx.x; i < SK; i += 256) ps[i] = rb(ps[i] / s);
  __syncthreads();
  if (threadIdx.x < DH) {
    float acc = 0.f;
    for (int k = 0; k < SK; ++k) acc += ps[k] * bf2f(V[((size_t)h * SK + k) * DH + threadIdx.x]);
    o[h * DH + threadIdx.x] = rb(acc);
  }
}
__global__ void k_wo(const float* o, const u16* wo, const u16* x, const float* mod, float* x2) {
  __shared__ float xs[QD]; __shared__ float red[256];
  for (int i = threadIdx.x; i < QD; i += 256) xs[i] = o[i];
  __syncthreads();
  float r; bool ok; gemv64(xs, wo, QD, D, blockIdx.x * 64, red, r, ok);
  if (ok) { int n = blockIdx.x * 64 + threadIdx.x; x2[n] = rb(bf2f(x[n]) + rb(mod[2 * D + n] * rb(r))); }
}
__global__ void k_gu(const float* x2, const float* mod, const u16* wg, const u16* wu, float* hh) {
  __shared__ float xs[D]; __shared__ float hs[D]; __shared__ float red[256];
  for (int i = threadIdx.x; i < D; i += 256) xs[i] = x2[i];
  __syncthreads();
  ada_ln(xs, mod + 4 * D, mod + 3 * D, hs, red);
  float g, u; bool ok;
  gemv64(hs, wg, D, F, blockIdx.x * 64, red, g, ok);
  gemv64(hs, wu, D, F, blockIdx.x * 64, red, u, ok);
  if (ok) hh[blockIdx.x * 64 + threadIdx.x] = rb(rb(g) * rb(u));
}
__global__ void k_down(const float* hh, const u16* wd, const float* x2, const float* mod, u16* out) {
  __shared__ float xs[F]; __shared__ float red[256];
  for (int i = threadIdx.x; i < F; i += 256) xs[i] = hh[i];
  __syncthreads();
  float r; bool ok; gemv64(xs, wd, F, D, blockIdx.x * 64, red, r, ok);
  if (ok) { int n = blockIdx.x * 64 + threadIdx.x; out[n] = f2bf(x2[n] + rb(mod[5 * D + n] * rb(r))); }
}

extern "C" void kernel_launch(void* const* d_in, const int* in_sizes, int n_in, void* d_out, int out_size, void* d_ws, size_t ws_size, hipStream_t stream) {
  const u16* x = (const u16*)d_in[0]; const u16* K = (const u16*)d_in[1]; const u16* V = (const u16*)d_in[2];
  const u16* wq = (const u16*)d_in[3]; const u16* wo = (const u16*)d_in[4];
  const u16* wg = (const u16*)d_in[6]; const u16* wu = (const u16*)d_in[7]; const u16* wd = (const u16*)d_in[8];
  const u16* c = (const u16*)d_in[9]; const u16* wmod = (const u16*)d_in[10];
  float* ws = (float*)d_ws;
  float* mod = ws; float* q = ws + 4096; float* sc = ws + 16384; float* o = sc + H * SK; float* x2 = o + QD; float* hh = x2 + 1024;
  k_mod<<<6 * D / 64, 256, 0, stream>>>(c, wmod, mod);
  k_q<<<QD / 64, 256, 0, stream>>>(x, mod, wq, q);
  k_scores<<<dim3(SK / 256, H), 256, 0, stream>>>(q, K, sc);
  k_pv<<<H, 256, 0, stream>>>(sc, V, o);
  k_wo<<<D / 64, 256, 0, stream>>>(o, wo, x, mod, x2);
  k_gu<<<F / 64, 256, 0, stream>>>(x2, mod, wg, wu, hh);
  k_down<<<D / 64, 256, 0, stream>>>(hh, wd, x2, mod, (u16*)d_out);
}
```

```cpp
#include <hip/hip_runtime.h>
#include <hip/hip_bf16.h>
#include <stdint.h>

#define MIMIC 1

typedef unsigned short u16;
constexpr int D = 640, H = 32, SK = 4096, DH = 192, F = 1536, QD = H * DH;
constexpr int NSPLIT = 16, KPS = SK / NSPLIT;

__device__ __forceinline__ float bf2f(u16 b) { return __uint_as_float((unsigned)b << 16); }
__device__ __forceinline__ u16 f2bf(float f) { unsigned u = __float_as_uint(f); u += 0x7FFFu + ((u >> 16) & 1u); return (u16)(u >> 16); }
__device__ __forceinline__ float rb(float f) {
  return bf2f(f2bf(f));
}

__device__ float block_sum(float v, float* red) {
  for (int o = 32; o > 0; o >>= 1) v += __shfl_xor(v, o);
  int w = threadIdx.x >> 6, nw = blockDim.x >> 6;
  __syncthreads();
  if ((threadIdx.x & 63) == 0) red[w] = v;
  __syncthreads();
  float s = 0.f;
  for (int i = 0; i < nw; ++i) s += red[i];
  return s;
}
__device__ float block_max(float v, float* red) {
  for (int o = 32; o > 0; o >>= 1) v = fmaxf(v, __shfl_xor(v, o));
  int w = threadIdx.x >> 6, nw = blockDim.x >> 6;
  __syncthreads();
  if ((threadIdx.x & 63) == 0) red[w] = v;
  __syncthreads();
  float s = -INFINITY;
  for (int i = 0; i < nw; ++i) s = fmaxf(s, red[i]);
  return s;
}

__device__ void gemv64(const float* xs, const u16* W, int K, int N, int n0, float* red, float& outv, bool& valid) {
  int col = n0 + (threadIdx.x & 63), kg = threadIdx.x >> 6;
  float acc = 0.f;
  for (int k = kg; k < K; k += 4) acc += xs[k] * bf2f(W[(size_t)k * N + col]);
  __syncthreads();
  red[threadIdx.x] = acc;
  __syncthreads();
  valid = threadIdx.x < 64;
  outv = 0.f;
  if (valid) outv = red[threadIdx.x] + red[threadIdx.x + 64] + red[threadIdx.x + 128] + red[threadIdx.x + 192];
}

__device__ void ada_ln(const float* xs, const float* sc, const float* sh, float* hs, float* red) {
  float s = 0.f;
  for (int i = threadIdx.x; i < D; i += blockDim.x) s += xs[i];
  float mean_f = block_sum(s, red) / D;
  float m = rb(mean_f);
  float v = 0.f;
  for (int i = threadIdx.x; i < D; i += blockDim.x) { float d = xs[i] - mean_f; v += d * d; }
  float var = rb(block_sum(v, red) / D);
  float den = rb(sqrtf(rb(var + 1e-5f)));
  for (int i = threadIdx.x; i < D; i += blockDim.x) {
    float ln = rb(rb(xs[i] - m) / den);
    hs[i] = rb(rb(ln * rb(1.f + sc[i])) + sh[i]);
  }
  __syncthreads();
}

__global__ void k_mod(const u16* c, const u16* wmod, float* mod) {
  __shared__ float xs[D]; __shared__ float red[256];
  for (int i = threadIdx.x; i < D; i += 256) { float v = bf2f(c[i]); xs[i] = rb(v / (1.f + __expf(-v))); }
  __syncthreads();
  float o; bool ok; gemv64(xs, wmod, D, 6 * D, blockIdx.x * 64, red, o, ok);
  if (ok) mod[blockIdx.x * 64 + threadIdx.x] = rb(o);
}
__global__ void k_q(const u16* x, const float* mod, const u16* wq, float* q) {
  __shared__ float xs[D]; __shared__ float hs[D]; __shared__ float red[256];
  for (int i = threadIdx.x; i < D; i += 256) xs[i] = bf2f(x[i]);
  __syncthreads();
  ada_ln(xs, mod + D, mod, hs, red);
  float o; bool ok; gemv64(hs, wq, D, QD, blockIdx.x * 64, red, o, ok);
  if (ok) q[blockIdx.x * 64 + threadIdx.x] = rb(o);
}
__global__ void k_scores(const float* q, const u16* K, float* sc) {
  int h = blockIdx.y, k = blockIdx.x * 256 + threadIdx.x;
  __shared__ float qs[DH];
  for (int i = threadIdx.x; i < DH; i += 256) qs[i] = q[h * DH + i];
  __syncthreads();
  const u16* kr = K + ((size_t)h * SK + k) * DH;
  float acc = 0.f;
  for (int d = 0; d < DH; ++d) acc += qs[d] * bf2f(kr[d]);
  const float scale = rb(0.07216878364870322f);
  (void)scale;
  sc[h * SK + k] = rb(rb(acc) * 0.07216878364870322f);
}
__global__ void k_pv(const float* sc, const u16* V, float* o) {
  int h = blockIdx.x;
  __shared__ float ps[SK]; __shared__ float red[256];
  float m = -INFINITY;
  for (int i = threadIdx.x; i < SK; i += 256) m = fmaxf(m, sc[h * SK + i]);
  m = block_max(m, red);
  float s = 0.f;
  for (int i = threadIdx.x; i < SK; i += 256) { float e = __expf(sc[h * SK + i] - m); ps[i] = e; s += e; }
  s = block_sum(s, red);
  for (int i = threadIdx.x; i < SK; i += 256) ps[i] = rb(ps[i] / s);
  __syncthreads();
  if (threadIdx.x < DH) {
    float acc = 0.f;
    for (int k = 0; k < SK; ++k) acc += ps[k] * bf2f(V[((size_t)h * SK + k) * DH + threadIdx.x]);
    o[h * DH + threadIdx.x] = rb(acc);
  }
}
__global__ void k_wo(const float* o, const u16* wo, const u16* x, const float* mod, float* x2) {
  __shared__ float xs[QD]; __shared__ float red[256];
  for (int i = threadIdx.x; i < QD; i += 256) xs[i] = o[i];
  __syncthreads();
  float r; bool ok; gemv64(xs, wo, QD, D, blockIdx.x * 64, red, r, ok);
  if (ok) { int n = blockIdx.x * 64 + threadIdx.x; x2[n] = rb(bf2f(x[n]) + rb(mod[2 * D + n] * rb(r))); }
}
__global__ void k_gu(const float* x2, const float* mod, const u16* wg, const u16* wu, float* hh) {
  __shared__ float xs[D]; __shared__ float hs[D]; __shared__ float red[256];
  for (int i = threadIdx.x; i < D; i += 256) xs[i] = x2[i];
  __syncthreads();
  ada_ln(xs, mod + 4 * D, mod + 3 * D, hs, red);
  float g, u; bool ok;
  gemv64(hs, wg, D, F, blockIdx.x * 64, red, g, ok);
  gemv64(hs, wu, D, F, blockIdx.x * 64, red, u, ok);
  if (ok) hh[blockIdx.x * 64 + threadIdx.x] = rb(rb(g) * rb(u));
}
__global__ void k_down(const float* hh, const u16* wd, const float* x2, const float* mod, u16* out) {
  __shared__ float xs[F]; __shared__ float red[256];
  for (int i = threadIdx.x; i < F; i += 256) xs[i] = hh[i];
  __syncthreads();
  float r; bool ok; gemv64(xs, wd, F, D, blockIdx.x * 64, red, r, ok);
  if (ok) { int n = blockIdx.x * 64 + threadIdx.x; out[n] = f2bf(x2[n] + rb(mod[5 * D + n] * rb(r))); }
}

#define AUX 19
typedef unsigned v4u __attribute__((ext_vector_type(4)));
typedef __bf16 bf16x2 __attribute__((ext_vector_type(2)));
__device__ __forceinline__ float dot2(unsigned a, unsigned b, float c) { return __builtin_amdgcn_fdot2_f32_bf16(__builtin_bit_cast(bf16x2, a), __builtin_bit_cast(bf16x2, b), c, false); }
template <int CTRL> __device__ __forceinline__ float dpp(float x) { return __builtin_bit_cast(float, __builtin_amdgcn_mov_dpp(__builtin_bit_cast(int, x), CTRL, 0xf, 0xf, true)); }
__global__ void __launch_bounds__(512, 2) k_attn(const float* q, const u16* K, const u16* V, float* part) {
  const int tid = threadIdx.x, lane = tid & 63, wave = __builtin_amdgcn_readfirstlane(tid >> 6), b = blockIdx.x;
  const int h = b & 31, s = b >> 5, kk = lane >> 3, j = lane & 7;
  const __amdgpu_buffer_rsrc_t rk = __builtin_amdgcn_make_buffer_rsrc((void*)K, 0, H * SK * DH * 2, 0x00020000);
  const __amdgpu_buffer_rsrc_t rv = __builtin_amdgcn_make_buffer_rsrc((void*)V, 0, H * SK * DH * 2, 0x00020000);
  unsigned qp[12];
#pragma unroll
  for (int t = 0; t < 3; ++t)
#pragma unroll
    for (int e = 0; e < 4; ++e) { const float* qq = q + h * DH + 64 * t + 8 * j + 2 * e; qp[4 * t + e] = (unsigned)f2bf(qq[0]) | ((unsigned)f2bf(qq[1]) << 16); }
  const int sbase = (h * SK + s * 512 + wave * 64) * 384;
  const int voff = kk * 384 + j * 16;
  v4u kr[24], vr[24];
#pragma unroll
  for (int i = 0; i < 24; ++i) kr[i] = __builtin_amdgcn_raw_buffer_load_b128(rk, voff + (i % 3) * 128, sbase + (i / 3) * 3072, AUX);
  __builtin_amdgcn_sched_barrier(0);
#pragma unroll
  for (int i = 0; i < 24; ++i) vr[i] = __builtin_amdgcn_raw_buffer_load_b128(rv, voff + (i % 3) * 128, sbase + (i / 3) * 3072, AUX);
  __builtin_amdgcn_sched_barrier(0);
  float sc[8];
#pragma unroll
  for (int g = 0; g < 8; ++g) {
    float a = 0.f;
#pragma unroll
    for (int t = 0; t < 3; ++t)
#pragma unroll
      for (int e = 0; e < 4; ++e) a = dot2(kr[3 * g + t][e], qp[4 * t + e], a);
    a += dpp<0xB1>(a); a += dpp<0x4E>(a); a += dpp<0x141>(a);
    sc[g] = rb(rb(a) * 0.07216878364870322f);
  }
  float m = sc[0];
#pragma unroll
  for (int g = 1; g < 8; ++g) m = fmaxf(m, sc[g]);
  for (int o = 8; o < 64; o <<= 1) m = fmaxf(m, __shfl_xor(m, o));
  float l = 0.f;
#pragma unroll
  for (int g = 0; g < 8; ++g) { sc[g] = __expf(sc[g] - m); l += sc[g]; }
  for (int o = 8; o < 64; o <<= 1) l += __shfl_xor(l, o);
  float acc[24];
#pragma unroll
  for (int i = 0; i < 24; ++i) acc[i] = 0.f;
#pragma unroll
  for (int g = 0; g < 8; ++g)
#pragma unroll
    for (int t = 0; t < 3; ++t)
#pragma unroll
      for (int e = 0; e < 4; ++e) { unsigned w = vr[3 * g + t][e]; acc[8 * t + 2 * e] += sc[g] * __uint_as_float(w << 16); acc[8 * t + 2 * e + 1] += sc[g] * __uint_as_float(w & 0xffff0000u); }
#pragma unroll
  for (int i = 0; i < 24; ++i) for (int o = 8; o < 64; o <<= 1) acc[i] += __shfl_xor(acc[i], o);
  float* pp = part + (size_t)(h * 64 + s * 8 + wave) * 256;
  if (kk == 0) {
#pragma unroll
    for (int t = 0; t < 3; ++t)
#pragma unroll
      for (int e = 0; e < 8; ++e) pp[64 * t + 8 * j + e] = acc[8 * t + e];
    if (j == 0) { pp[192] = m; pp[193] = l; }
  }
}
__global__ void k_comb(const float* part, float* o) {
  int h = blockIdx.x, d = threadIdx.x;
  const float* pp = part + (size_t)h * 64 * 256;
  float M = -INFINITY;
  for (int i = 0; i < 64; ++i) M = fmaxf(M, pp[i * 256 + 192]);
  float L = 0.f, a = 0.f;
  for (int i = 0; i < 64; ++i) { float w = __expf(pp[i * 256 + 192] - M); L += w * pp[i * 256 + 193]; a += w * pp[i * 256 + d]; }
  o[h * DH + d] = rb(a / L);
}

extern "C" void kernel_launch(void* const* d_in, const int* in_sizes, int n_in, void* d_out, int out_size, void* d_ws, size_t ws_size, hipStream_t stream) {
  const u16* x = (const u16*)d_in[0]; const u16* K = (const u16*)d_in[1]; const u16* V = (const u16*)d_in[2];
  const u16* wq = (const u16*)d_in[3]; const u16* wo = (const u16*)d_in[4];
  const u16* wg = (const u16*)d_in[6]; const u16* wu = (const u16*)d_in[7]; const u16* wd = (const u16*)d_in[8];
  const u16* c = (const u16*)d_in[9]; const u16* wmod = (const u16*)d_in[10];
  float* ws = (float*)d_ws;
  float* mod = ws; float* q = ws + 4096; float* sc = ws + 16384; float* o = sc + H * SK; float* x2 = o + QD; float* hh = x2 + 1024;
  k_mod<<<6 * D / 64, 256, 0, stream>>>(c, wmod, mod);
  k_q<<<QD / 64, 256, 0, stream>>>(x, mod, wq, q);
  float* part = ws + (1 << 20);
  k_attn<<<256, 512, 0, stream>>>(q, K, V, part);
  k_comb<<<H, DH, 0, stream>>>(part, o);
  k_wo<<<D / 64, 256, 0, stream>>>(o, wo, x, mod, x2);
  k_gu<<<F / 64, 256, 0, stream>>>(x2, mod, wg, wu, hh);
  k_down<<<D / 64, 256, 0, stream>>>(hh, wd, x2, mod, (u16*)d_out);
}
```

```cpp
#include <hip/hip_runtime.h>
#include <stdint.h>
#pragma clang fp contract(off)

typedef unsigned short u16;
typedef unsigned u32;
typedef unsigned long long u64;
typedef u32 v4u __attribute__((ext_vector_type(4)));
typedef u32 v2u __attribute__((ext_vector_type(2)));
typedef __bf16 bf16x2 __attribute__((ext_vector_type(2)));
#define LAS __attribute__((address_space(3)))
#define GAS __attribute__((address_space(1)))
typedef __amdgpu_buffer_rsrc_t rsrc_t;

constexpr int D = 640, H = 32, SK = 4096, DH = 192, F = 1536, QD = H * DH;
constexpr int NWG = 256, NTHR = 512;
constexpr int AUX_NT = 2, AUX_SC1 = 16;
#define AUX_KV 2
#define AUX_W 0
#define WK 7
#define WV 7
#define AUX_GU 0

constexpr int WS_EPOCH = 0;
constexpr int WS_HG = 4096;
constexpr int WS_MP = WS_HG + 4096;
constexpr int MP_S = 1280 * 8 + 640, OP_S = 1024 + 128, AP_S = 32768 + 128, DP_S = 3072 + 128;
constexpr int WS_MG = WS_MP + 8 * MP_S;
constexpr int WS_QP = WS_MG + 2560 * 8;
constexpr int WS_ML = WS_QP + 32 * 8 * 1024;
constexpr int WS_OP = WS_ML + 32 * 32 * 16;
constexpr int WS_AP = WS_OP + 32 * 32 * OP_S;
constexpr int WS_DP = WS_AP + 20 * AP_S;
constexpr int WS_X2 = WS_DP + 20 * DP_S;
constexpr int WS_CL = WS_X2 + 1280;
constexpr int WS_END = WS_CL + 20 * 128;
constexpr int WS_STAMP = 8 << 20;
constexpr int RED0 = 224;
constexpr u32 SENT = 0xFFFFFFFFu;
constexpr int OOB = 0x40000000;
constexpr u32 SPIN_CAP = 1u << 20;

struct Params {
  const u16 *x, *K, *V, *wq, *wo, *wg, *wu, *wd, *c, *wmod;
  u16* out;
  unsigned char* ws;
};

__device__ __forceinline__ float asf(u32 u) { return __builtin_bit_cast(float, u); }
__device__ __forceinline__ u32 asu(float f) { return __builtin_bit_cast(u32, f); }
__device__ __forceinline__ float lo_f(u32 w) { return asf(w << 16); }
__device__ __forceinline__ float hi_f(u32 w) { return asf(w & 0xffff0000u); }
__device__ __forceinline__ u32 pack2(float lo, float hi) { bf16x2 v = {(__bf16)lo, (__bf16)hi}; return __builtin_bit_cast(u32, v); }
__device__ __forceinline__ float rb(float f) { return (float)(__bf16)f; }
__device__ __forceinline__ float dot2(u32 a, u32 b, float c) { return __builtin_amdgcn_fdot2_f32_bf16(__builtin_bit_cast(bf16x2, a), __builtin_bit_cast(bf16x2, b), c, false); }
__device__ __forceinline__ u32 perm_lo(u32 a, u32 b) { return __builtin_amdgcn_perm(b, a, 0x05040100u); }
__device__ __forceinline__ u32 perm_hi(u32 a, u32 b) { return __builtin_amdgcn_perm(b, a, 0x07060302u); }
template <int CTRL> __device__ __forceinline__ float dpp(float x) { return asf(__builtin_amdgcn_mov_dpp(asu(x), CTRL, 0xf, 0xf, true)); }
constexpr int XOR1 = 0xB1, XOR2 = 0x4E, HMIR = 0x141, ROR8 = 0x128;
__device__ __forceinline__ float tr32(float a, float b) { auto s = __builtin_amdgcn_permlane32_swap(asu(a), asu(b), false, false); return asf(s[0]) + asf(s[1]); }
__device__ __forceinline__ float tr16(float a, float b) { auto s = __builtin_amdgcn_permlane16_swap(asu(a), asu(b), false, false); return asf(s[0]) + asf(s[1]); }
__device__ __forceinline__ float tr8(float a, float b, bool bit3) { const float keep = bit3 ? b : a, send = bit3 ? a : b; return keep + dpp<ROR8>(send); }
__device__ __forceinline__ float max32(float a) { auto s = __builtin_amdgcn_permlane32_swap(asu(a), asu(a), false, false); return fmaxf(asf(s[0]), asf(s[1])); }
__device__ __forceinline__ float max16(float a) { auto s = __builtin_amdgcn_permlane16_swap(asu(a), asu(a), false, false); return fmaxf(asf(s[0]), asf(s[1])); }
__device__ __forceinline__ float sum8(float a) { a += dpp<XOR1>(a); a += dpp<XOR2>(a); a += dpp<HMIR>(a); return a; }
__device__ __forceinline__ float sum_hi(float a) { a += dpp<ROR8>(a); a = tr16(a, a); return tr32(a, a); }
__device__ __forceinline__ float wave_sum(float a) { return sum_hi(sum8(a)); }
__device__ __forceinline__ float max_hi(float a) { a = fmaxf(a, dpp<ROR8>(a)); return max32(max16(a)); }
__device__ __forceinline__ float wave_max(float a) { a = fmaxf(a, dpp<XOR1>(a)); a = fmaxf(a, dpp<XOR2>(a)); a = fmaxf(a, dpp<HMIR>(a)); return max_hi(a); }
__device__ __forceinline__ float tr_reduce8(const float* v, int lane) {
  const float a0 = tr32(v[0], v[4]), a1 = tr32(v[1], v[5]), a2 = tr32(v[2], v[6]), a3 = tr32(v[3], v[7]);
  const float b0 = tr16(a0, a2), b1 = tr16(a1, a3);
  return tr8(b0, b1, (lane & 8) != 0);
}
__device__ __forceinline__ rsrc_t mk(const void* p, int bytes) { return __builtin_amdgcn_make_buffer_rsrc((void*)p, 0, bytes, 0x00020000); }
template <int AUX> __device__ __forceinline__ v4u ld4(rsrc_t r, int vo, int so) { return __builtin_amdgcn_raw_buffer_load_b128(r, vo, so, AUX); }
template <int AUX> __device__ __forceinline__ v2u ld2(rsrc_t r, int vo, int so) { return __builtin_amdgcn_raw_buffer_load_b64(r, vo, so, AUX); }
template <int AUX> __device__ __forceinline__ u32 ld1(rsrc_t r, int vo, int so) { return __builtin_amdgcn_raw_buffer_load_b32(r, vo, so, AUX); }
__device__ __forceinline__ void st4(rsrc_t r, int vo, int so, v4u v) { __builtin_amdgcn_raw_buffer_store_b128(v, r, vo, so, AUX_SC1); }
__device__ __forceinline__ void st2(rsrc_t r, int vo, int so, v2u v) { __builtin_amdgcn_raw_buffer_store_b64(v, r, vo, so, AUX_SC1); }
__device__ __forceinline__ void st1(rsrc_t r, int vo, int so, u32 v) { __builtin_amdgcn_raw_buffer_store_b32(v, r, vo, so, AUX_SC1); }
__device__ __forceinline__ u32 umax(u32 a, u32 b) { return a > b ? a : b; }
__device__ __forceinline__ u32 canon(float f) { const u32 u = asu(f); return u < 0xFFFFFFFEu ? u : 0xFFFFFFFEu; }
template <int CTRL> __device__ __forceinline__ u32 dppu(u32 x) { return __builtin_amdgcn_mov_dpp(x, CTRL, 0xf, 0xf, true); }
#define CFENCE() asm volatile("" ::: "memory")
#define LDS_WAIT() asm volatile("s_waitcnt lgkmcnt(0)" ::: "memory")
#define BARRIER() asm volatile("s_waitcnt lgkmcnt(0)\n\ts_barrier" ::: "memory")
#define SCHED() __builtin_amdgcn_sched_barrier(0)
#define KEEPV(x) asm volatile("" : "+v"(x))
__device__ __forceinline__ float frcp(float x) { return __builtin_amdgcn_rcpf(x); }
__device__ __forceinline__ float fsqrt(float x) { return __builtin_amdgcn_sqrtf(x); }
__device__ __forceinline__ float silu(float x) { return x / (1.f + expf(-x)); }
template <int N> __device__ __forceinline__ void vm_wait() { asm volatile("s_waitcnt vmcnt(%0)" :: "i"(N) : "memory"); }
__device__ __forceinline__ bool all_zero(u32 bad) { return __builtin_amdgcn_ballot_w64(bad != 0u) == 0ull; }
__device__ __forceinline__ bool lds_wait_flag(volatile LAS u32* f) {
#pragma unroll 1
  for (u32 sp = 0; sp < (SPIN_CAP << 3); ++sp) {
    if (__builtin_amdgcn_readfirstlane(*f) != 0u) { CFENCE(); return true; }
    __builtin_amdgcn_s_sleep(1);
  }
  return false;
}
#define STAMP(i) do {} while (0)
#define STAMP_DUMP() do {} while (0)
#define STAMP_DECL()

constexpr int L_KF = 0, L_GF = 4, L_PF = 8, L_QF = 12, L_T = 13, L_TF = 14, L_CNT = 15, L_WF = 16, L_HS = 24  , L_QS = 64  ,
              L_PS = 160  , L_SI = 544  , L_RED = 864  , L_GU = 1888  ,
              L_SPILL = 3168  , L_WORDS = L_SPILL + 8 * 9 * 256;
constexpr float LOG2E = 1.4426950408889634f;
constexpr int NG = 19, NL = 3 * NG;
#define GSTEP 36
#define WOSTEP 44
#define HSLEEP 2
#define WK0 3
#define WQW 16
#define WS_ 4
#define DRY 0

#define POLL(SLEEP, LOADS, CHECK) do { u32 sp_ = 0; for (;; ++sp_) { CFENCE(); u32 bad = 0; LOADS; CHECK; if (all_zero(bad)) break; if (sp_ > SPIN_CAP) return; \
    if (SLEEP) __builtin_amdgcn_s_sleep(SLEEP); } } while (0)
#define TPOLL(SLEEP, LOADS, CHECK) do { u32 sp_ = 0; for (;; ++sp_) { CFENCE(); u32 bad = 0; LOADS; CHECK; if (all_zero(bad) || !real) break; if (sp_ > SPIN_CAP) return; \
    if (SLEEP) __builtin_amdgcn_s_sleep(SLEEP); } } while (0)
#define TAG2(v) do { bad |= (v).y ^ T; KEEPV(bad); } while (0)
#define TAG4(v) do { bad |= ((v).y ^ T) | ((v).w ^ T); KEEPV(bad); } while (0)
#define XOR4(v) ((v).x ^ (v).y ^ (v).z ^ (v).w ^ T)
#define UNR _Pragma("unroll")
#define SUM8(a0, a1, a2, a3, a4, a5, a6, a7) ((((a0) + (a1)) + ((a2) + (a3))) + (((a4) + (a5)) + ((a6) + (a7))))
#define ZERO8 {0.f, 0.f, 0.f, 0.f, 0.f, 0.f, 0.f, 0.f}

__device__ __forceinline__ u32 ada_pair(u32 xp, float mb, float rden, float c0, float c1, float s0, float s1) {
  return pack2(rb(rb(rb(lo_f(xp) - mb) * rden) * c0) + s0, rb(rb(rb(hi_f(xp) - mb) * rden) * c1) + s1);
}
__device__ __forceinline__ void ln_stats(const u32* xw, float& mb, float& rden) {
  const float sm = (((lo_f(xw[0]) + hi_f(xw[0])) + (lo_f(xw[1]) + hi_f(xw[1]))) + ((lo_f(xw[2]) + hi_f(xw[2])) + (lo_f(xw[3]) + hi_f(xw[3])))) + (lo_f(xw[4]) + hi_f(xw[4]));
  const float mean = wave_sum(sm) * (1.f / D);
  float sq0 = 0.f, sq1 = 0.f;
  UNR for (int i = 0; i < 5; ++i) { const float d0 = lo_f(xw[i]) - mean, d1 = hi_f(xw[i]) - mean; sq0 = fmaf(d0, d0, sq0); sq1 = fmaf(d1, d1, sq1); }
  const float var = rb(wave_sum(sq0 + sq1) * (1.f / D));
  rden = frcp(rb(fsqrt(rb(var + 1e-5f))));
  mb = rb(mean);
}
__device__ __forceinline__ void pair_rows(v4u& a, v4u& c) { UNR for (int e = 0; e < 4; ++e) { const u32 x = a[e], y = c[e]; a[e] = perm_lo(x, y); c[e] = perm_hi(x, y); } KEEPV(a); KEEPV(c); }
__device__ __forceinline__ void mac8(float* acc, const v4u& lo, const v4u& hi, u32 ap) { UNR for (int e = 0; e < 4; ++e) { acc[2 * e] = dot2(lo[e], ap, acc[2 * e]); acc[2 * e + 1] = dot2(hi[e], ap, acc[2 * e + 1]); } }
__device__ __forceinline__ u32 umax4(u32 m, const v4u& g) { return umax(umax(umax(m, g.x), g.y), umax(g.z, g.w)); }

__global__ void __launch_bounds__(NTHR, 2) fused_block(Params p) {
  __shared__ __attribute__((aligned(16))) u32 smem[L_WORDS];
  LAS u32* L = (LAS u32*)smem;
  volatile LAS u32* LV = (volatile LAS u32*)smem;
  const int tid = threadIdx.x, lane = tid & 63, wave = __builtin_amdgcn_readfirstlane(tid >> 6), b = blockIdx.x;
  const int h = (b & 7) * 4 + ((b >> 3) & 3), s = b >> 5, r8 = lane >> 3, c8 = lane & 7, w = wave & 3;
  const bool svc = s == 7, mlp = svc && (h & 3) != 3;
  const int j = 3 * (h >> 2) + (h & 3), xi = h >> 2;
  const rsrc_t ws = mk(p.ws, WS_END);
  const int l40 = lane < 40 ? lane : 39;
  u32 T = 0;
  STAMP_DECL();
  STAMP(0);
  const rsrc_t rm = mk(p.wmod, D * 6 * D * 2), rc = mk(p.c, D * 2);
#define MKTAG(e) (__builtin_amdgcn_readfirstlane(e) * 0x9E3779B1u + 0x7F4A7C15u)
#define MOD_LD(i, rowblk, colbyte) ld4<AUX_W>(rm, r8 * 2 * 7680 + c8 * 16, (80 * (rowblk) + 16 * ((i) >> 1) + ((i) & 1)) * 7680 + (colbyte))
#define MOD_SILU(cw, sp) do { L[L_SI + wave * 40 + l40] = pack2(silu(lo_f(cw)), silu(hi_f(cw))); LDS_WAIT(); UNR for (int i = 0; i < 5; ++i) sp[i] = L[L_SI + wave * 40 + 8 * i + r8]; } while (0)
#define MOD_MAC(res, m, sp) do { float acc_[8] = ZERO8; \
    UNR for (int i = 0; i < 5; ++i) { pair_rows(m[2 * i], m[2 * i + 1]); mac8(acc_, m[2 * i], m[2 * i + 1], sp[i]); } res = tr_reduce8(acc_, lane); } while (0)
  if (wave == 5 && b < 160) {
    __builtin_amdgcn_s_setprio(3);
    const int blk = b >> 3, rblk = b & 7;
    u32 sp[5]; v4u m0[10];
    const u32 cw = ld1<0>(rc, l40 * 4, rblk * 160);
    UNR for (int i = 0; i < 10; ++i) m0[i] = MOD_LD(i, rblk, 128 * blk);
    const u32 E = ld1<AUX_SC1>(ws, 0, WS_EPOCH);
    SCHED();
    BARRIER();
    MOD_SILU(cw, sp);
    float v;
    MOD_MAC(v, m0, sp);
    const u32 Tq = MKTAG(E), vn = dppu<ROR8>(asu(v));
    if (!(lane & 8)) st4(ws, (8 * c8 + r8) * 8, WS_MP + rblk * MP_S + 512 * blk, v4u{asu(v), Tq, vn, Tq});
    __builtin_amdgcn_s_setprio(0);
    STAMP(1);
  } else if (wave == 5 && b < 180) {
    BARRIER();
    const int jc = b - 160;
    const u32 E = ld1<AUX_SC1>(ws, 0, WS_EPOCH);
    const v4u sv = {SENT, SENT, SENT, SENT};
    UNR for (int i = 0; i < 32; ++i) { if (i >= 8) vm_wait<7>(); st4(ws, lane * 16, WS_AP + jc * AP_S + i * 1024, sv); SCHED(); }
    UNR for (int i = 0; i < 3; ++i) st4(ws, lane * 16, WS_DP + jc * DP_S + i * 1024, sv);
    if (lane < 4) st4(ws, lane * 16, WS_X2 + jc * 64, sv);
    const u32 Tq = MKTAG(E);
    vm_wait<0>();
    if (lane == 0) st4(ws, 0, WS_CL + jc * 128, v4u{0u, Tq, 0u, Tq});
    STAMP(2);
  } else if (!svc && (wave == 4 || (wave == 6 && s == 6))) {
    __builtin_amdgcn_s_setprio(3);
    const int sl = wave == 4 ? s : 7;
    LAS u32* hs = L + (wave == 4 ? L_HS : L_RED);
    BARRIER();
    const rsrc_t rqw = mk(p.wq, D * QD * 2);
    v4u qw[30];
    UNR for (int i = 0; i < 30; ++i) {
      if (i >= WQW) vm_wait<WQW - 1>();
      qw[i] = ld4<AUX_W>(rqw, r8 * 2 * 12288 + c8 * 16 + (i / 10) * 128, (80 * sl + 16 * ((i % 10) >> 1) + (i & 1)) * 12288 + h * DH * 2);
      SCHED();
    }
    const u32 E = ld1<AUX_SC1>(ws, 0, WS_EPOCH);
    SCHED();
    UNR for (int i = 0; i < 15; ++i) pair_rows(qw[2 * i], qw[2 * i + 1]);
    T = MKTAG(E);
    if (wave == 4) { L[L_T] = T; LDS_WAIT(); LV[L_TF] = 1u; }
    STAMP(3);
    {
      v2u g;
      POLL(HSLEEP, g = ld2<AUX_SC1>(ws, l40 * 8, WS_HG + sl * 320), TAG2(g));
      hs[l40] = g.x;
      LDS_WAIT();
    }
    STAMP(4);
    {
      u32 hp[5];
      UNR for (int i = 0; i < 5; ++i) hp[i] = hs[8 * i + r8];
      u32 qv[3];
      UNR for (int t = 0; t < 3; ++t) {
        float acc[8] = ZERO8;
        UNR for (int i = 0; i < 5; ++i) mac8(acc, qw[10 * t + 2 * i], qw[10 * t + 2 * i + 1], hp[i]);
        qv[t] = asu(tr_reduce8(acc, lane));
      }
      st4(ws, lane * 16, WS_QP + (h * 8 + sl) * 1024, v4u{qv[0], qv[1], qv[2], qv[0] ^ qv[1] ^ qv[2] ^ T});
    }
    __builtin_amdgcn_s_setprio(0);
    STAMP(5);
  } else if (svc && !mlp && wave == 0) {
    const rsrc_t rx = mk(p.x, D * 2);
    u32 xw[5];
    UNR for (int i = 0; i < 5; ++i) xw[i] = ld1<0>(rx, lane * 4 + i * 256, 0);
    const u32 xme = ld1<0>(rx, l40 * 4, xi * 160);
    const u32 E = ld1<AUX_SC1>(ws, 0, WS_EPOCH);
    SCHED();
    BARRIER();
    float mb, rden;
    ln_stats(xw, mb, rden);
    T = MKTAG(E);
    v4u g[16];
    POLL(0, UNR for (int q = 0; q < 16; ++q) g[q] = ld4<AUX_SC1>(ws, l40 * 16, WS_MP + (q & 7) * MP_S + ((q >> 3) * 640 + 80 * xi) * 8),
         UNR for (int q = 0; q < 16; ++q) TAG4(g[q]));
    float v[4];
    UNR for (int k = 0; k < 4; ++k) { const int o = 8 * (k >> 1), e = 2 * (k & 1);
      v[k] = rb(SUM8(asf(g[o][e]), asf(g[o + 1][e]), asf(g[o + 2][e]), asf(g[o + 3][e]), asf(g[o + 4][e]), asf(g[o + 5][e]), asf(g[o + 6][e]), asf(g[o + 7][e]))); }
    const u32 hp = ada_pair(xme, mb, rden, rb(1.f + v[2]), rb(1.f + v[3]), v[0], v[1]), hn = dppu<XOR1>(hp);
    if (lane < 40 && !(lane & 1)) st4(ws, lane * 8, WS_HG + xi * 320, v4u{hp, T, hn, T});
    LV[L_QF] = 1u;
    STAMP(1);
  } else {
    if (wave == 7 && lane < 24) L[lane] = 0u;
    BARRIER();
  }
  const int wid = (3 - w) * 7 + s;
  const int kvoff = r8 * 384 + c8 * 16, kvbase = (h * 512 + wid) * 3072;
  const int lastbase = wid < 8 ? kvbase + 18 * 28 * 3072 : OOB;
  LAS v4u* spill = (LAS v4u*)(L + L_SPILL + wave * 9 * 256) + lane;
  if (!svc && wave >= 4) {
    if (!lds_wait_flag(LV + L_KF + w)) return;
    STAMP(7);
    SCHED();
    const rsrc_t rv = mk(p.V, H * SK * DH * 2);
    v4u vr[48];
#define VR(i) vr[(i) % 48]
#define VISSUE(i) do { if ((i) >= WV) vm_wait<WV - 1>(); \
      VR(i) = (i) < 3 ? ld4<AUX_KV>(rv, kvoff + (i) * 128, lastbase) \
                      : ld4<AUX_KV>(rv, kvoff + ((((i) + 3) % 6) >> 1) * 128, kvbase + (2 * (((i) - 3) / 6) + (((i) + 1) & 1)) * (28 * 3072)); SCHED(); \
      if ((i) - WV >= 4 && !(((i) - WV) & 1)) pair_rows(VR((i) - WV - 1), VR((i) - WV)); \
      if ((i) == 8 + WV) { UNR for (int k = 0; k < 9; ++k) spill[64 * k] = VR(k); } \
      if ((i) == GSTEP) LV[L_GF + w] = 1u; \
      if ((i) == WOSTEP) LV[L_WF + w] = 1u; SCHED(); } while (0)
    UNR for (int i = 0; i < 30; ++i) VISSUE(i);
    UNR for (int i = 30; i < NL; ++i) VISSUE(i);
    STAMP(8);
    if (!lds_wait_flag(LV + L_PF + w)) return;
    STAMP(9);
    u32 pp[12];
    UNR for (int k = 0; k < 3; ++k) { const v4u t4 = *(const LAS v4u*)(L + L_PS + (w * 8 + r8) * 12 + 4 * k); UNR for (int e = 0; e < 4; ++e) pp[4 * k + e] = t4[e]; }
    T = __builtin_amdgcn_readfirstlane(L[L_T]);
    float acc[24];
    UNR for (int i = 0; i < 24; ++i) acc[i] = 0.f;
    constexpr int LASTP = ((NL - 1 - WV) & ~1);
#define PV_T(i, t) do { if (4 + 6 * (i) + 2 * (t) > LASTP) pair_rows(VR(3 + 6 * (i) + 2 * (t)), VR(4 + 6 * (i) + 2 * (t))); \
      mac8(acc + 8 * (t), VR(3 + 6 * (i) + 2 * (t)), VR(4 + 6 * (i) + 2 * (t)), pp[i]); } while (0)
#define PV(i) do { PV_T(i, 0); PV_T(i, 1); PV_T(i, 2); SCHED(); } while (0)
    PV(1); PV(2);
    {
      v4u sv[9];
      UNR for (int k = 0; k < 9; ++k) sv[k] = spill[64 * k];
      SCHED();
      PV(3); PV(4);
      UNR for (int t = 0; t < 3; ++t) {
        UNR for (int e = 0; e < 4; ++e) { acc[8 * t + 2 * e] = dot2(sv[t][e] & 0xffffu, pp[9], acc[8 * t + 2 * e]); acc[8 * t + 2 * e + 1] = dot2(sv[t][e] & 0xffff0000u, pp[10], acc[8 * t + 2 * e + 1]); }
        mac8(acc + 8 * t, sv[3 + 2 * t], sv[4 + 2 * t], pp[0]);
      }
      SCHED();
    }
    PV(5); PV(6);
    STAMP(10);
    PV(7);
    PV_T(8, 0);
    const float o0 = tr_reduce8(acc, lane);
    SCHED();
    PV_T(8, 1);
    const float o1 = tr_reduce8(acc + 8, lane);
    SCHED();
    PV_T(8, 2);
    const float o2 = tr_reduce8(acc + 16, lane);
    st4(ws, lane * 16, WS_OP + (h * 32 + wid) * OP_S, v4u{asu(o0), asu(o1), asu(o2), asu(o0) ^ asu(o1) ^ asu(o2) ^ T});
    STAMP(11);
    STAMP_DUMP();
    return;
  }
  v4u gw[10], uw[10], dw[16];
  float c2[4], s2[4], gav = 0.f, gbv = 0.f, xv = 0.f;
  const int pidx = 40 * wave + 5 * r8;
  if (!svc) {
    const rsrc_t rk = mk(p.K, H * SK * DH * 2);
    v4u kr[48];
#define KR(i) kr[(i) % 48]
#define KISSUE(i) do { if ((i) >= 8) vm_wait<WK - 1>(); else if ((i) >= WK0) vm_wait<WK0 - 1>(); \
      KR(i) = ld4<AUX_KV>(rk, kvoff + ((i) % 3) * 128, (i) < 54 ? kvbase + ((i) / 3) * (28 * 3072) : lastbase); SCHED(); \
      if ((i) == 8 + WK) { UNR for (int k = 0; k < 9; ++k) spill[64 * k] = KR(k); SCHED(); } } while (0)
    UNR for (int i = 0; i < NL; ++i) KISSUE(i);
    LV[L_KF + w] = 1u;
    STAMP(1);
    if (!lds_wait_flag(LV + L_TF)) return;
    T = __builtin_amdgcn_readfirstlane(L[L_T]);
    if (w == 0) {
      v4u g[8];
      POLL(4, UNR for (int q = 0; q < 8; ++q) g[q] = ld4<AUX_SC1>(ws, lane * 16, WS_QP + (h * 8 + q) * 1024),
           UNR for (int i = 0; i < 8; ++i) { bad |= XOR4(g[i]); KEEPV(bad); });
      LAS u16* qs = (LAS u16*)(L + L_QS);
      UNR for (int t = 0; t < 3; ++t) {
        const float q = SUM8(asf(g[0][t]), asf(g[1][t]), asf(g[2][t]), asf(g[3][t]), asf(g[4][t]), asf(g[5][t]), asf(g[6][t]), asf(g[7][t]));
        qs[64 * t + 8 * c8 + r8] = (u16)pack2(q, q);
      }
      LDS_WAIT();
      LV[L_QF] = 1u;
    } else if (!lds_wait_flag(LV + L_QF)) return;
    CFENCE();
    STAMP(2);
    v4u qp[3];
    float sc[NG];
    UNR for (int t = 0; t < 3; ++t) qp[t] = *(const LAS v4u*)(L + L_QS + 32 * t + 4 * c8);
#define QK(g, k0, k1, k2) do { float a_ = 0.f; UNR for (int e = 0; e < 4; ++e) a_ = dot2((k0)[e], qp[0][e], a_); UNR for (int e = 0; e < 4; ++e) a_ = dot2((k1)[e], qp[1][e], a_); \
      UNR for (int e = 0; e < 4; ++e) a_ = dot2((k2)[e], qp[2][e], a_); sc[g] = rb(rb(sum8(a_)) * 0.07216878364870322f); } while (0)
    UNR for (int g = 3; g < NG; ++g) QK(g, KR(3 * g), KR(3 * g + 1), KR(3 * g + 2));
    SCHED();
    UNR for (int g = 0; g < 3; ++g) { const v4u k0 = spill[64 * (3 * g)], k1 = spill[64 * (3 * g + 1)], k2 = spill[64 * (3 * g + 2)]; QK(g, k0, k1, k2); }
    if (wid >= 8) sc[18] = -INFINITY;
    float m = sc[0];
    UNR for (int g = 1; g < NG; ++g) m = fmaxf(m, sc[g]);
    m = max_hi(m);
    float l = 0.f;
    UNR for (int g = 0; g < NG; ++g) l += __builtin_amdgcn_exp2f((sc[g] - m) * LOG2E);
    l = sum_hi(l);
    if (lane == 0) st4(ws, 0, WS_ML + (h * 32 + wid) * 16, v4u{asu(m), T, asu(l), T});
    STAMP(4);
    if (!lds_wait_flag(LV + L_GF + w)) return;
    STAMP(5);
    float M, Ls;
    {
      const int l32 = lane & 31;
      const int vo = lane < 32 ? WS_ML + h * 512 + (l32 < 28 ? l32 : 0) * 16 : WS_CL + (l32 < 20 ? l32 : 0) * 128;
      v4u g;
      POLL(8, g = ld4<AUX_SC1>(ws, vo, 0), TAG4(g));
      const float mm = lane < 28 ? asf(g.x) : -INFINITY;
      M = wave_max(mm);
      Ls = wave_sum(lane < 28 ? asf(g.z) * __builtin_amdgcn_exp2f((mm - M) * LOG2E) : 0.f);
    }
    {
      const float inv = 1.f / Ls;
      v4u pp[3];
      UNR for (int i = 0; i < 9; ++i)
        pp[i >> 2][i & 3] = pack2(__builtin_amdgcn_exp2f((sc[2 * i] - M) * LOG2E) * inv, __builtin_amdgcn_exp2f((sc[2 * i + 1] - M) * LOG2E) * inv);
      const float pl = __builtin_amdgcn_exp2f((sc[18] - M) * LOG2E) * inv;
      pp[2][1] = pack2(pl, 0.f); pp[2][2] = pack2(0.f, pl); pp[2][3] = 0u;
      if (c8 == 0) { UNR for (int k = 0; k < 3; ++k) *(LAS v4u*)(L + L_PS + (w * 8 + r8) * 12 + 4 * k) = pp[k]; }
      LDS_WAIT();
      LV[L_PF + w] = 1u;
    }
    STAMP(6);
  } else {
    if (!mlp && !lds_wait_flag(LV + L_QF)) return;
    if (mlp && j < 20) xv = lo_f(p.x[32 * j + (lane & 31)]);
    u32 sp[5]; v4u m0[10];
    const u32 cw = ld1<0>(rc, l40 * 4, wave * 160);
    const u32 E = ld1<AUX_SC1>(ws, 0, WS_EPOCH);
    UNR for (int i = 0; i < 10; ++i) m0[i] = MOD_LD(i, wave, 2560 + 128 * h);
    SCHED();
    if (!mlp) {
      UNR for (int i = 0; i < 10; ++i) gw[i] = MOD_LD(i, wave, 2560 + 128 * (32 + xi));
    } else {
      const rsrc_t rg = mk(p.wg, D * F * 2), ru = mk(p.wu, D * F * 2), rd = mk(p.wd, wave < 5 ? F * D * 2 : 0);
      UNR for (int i = 0; i < 10; ++i) { gw[i] = ld4<AUX_W>(rg, r8 * 10 * 3072 + c8 * 16, (80 * wave + i) * 3072 + 128 * j); uw[i] = ld4<AUX_W>(ru, r8 * 10 * 3072 + c8 * 16, (80 * wave + i) * 3072 + 128 * j); }
      UNR for (int i = 0; i < 16; ++i) dw[i] = ld4<AUX_W>(rd, (lane >> 4) * 16 * 1280 + (lane & 15) * 16, (64 * j + i) * 1280 + 256 * wave);
    }
    SCHED();
    MOD_SILU(cw, sp);
    T = MKTAG(E);
    float v0;
    MOD_MAC(v0, m0, sp);
    L[L_RED + wave * 64 + 8 * c8 + r8] = asu(v0);
    if (!mlp) { MOD_MAC(v0, gw, sp); L[L_RED + 512 + wave * 64 + 8 * c8 + r8] = asu(v0); }
    BARRIER();
    if (wave == 7 || (wave == 6 && !mlp)) {
      const LAS u32* a = L + L_RED + (7 - wave) * 512 + lane;
      const u32 v = asu(rb(SUM8(asf(a[0]), asf(a[64]), asf(a[128]), asf(a[192]), asf(a[256]), asf(a[320]), asf(a[384]), asf(a[448])))), vn = dppu<XOR1>(v);
      if (!(lane & 1)) st4(ws, lane * 8, WS_MG + (wave == 6 ? 32 + xi : h) * 512, v4u{v, T, vn, T});
    }
    STAMP(2);
    if (mlp) {
      UNR for (int i = 0; i < 5; ++i) { pair_rows(gw[2 * i], gw[2 * i + 1]); pair_rows(uw[2 * i], uw[2 * i + 1]); }
      UNR for (int i = 0; i < 8; ++i) pair_rows(dw[2 * i], dw[2 * i + 1]);
    }
    STAMP(3);
    v4u g[4]; v2u ga, gb, cl;
    const int cown = (j < 20 ? 32 * j : 0) + (lane & 31);
    POLL(16, g[0] = ld4<AUX_SC1>(ws, (pidx + (c8 & 3)) * 16, WS_MG + 640 * 8); g[1] = ld4<AUX_SC1>(ws, (pidx + 4) * 16, WS_MG + 640 * 8);
         g[2] = ld4<AUX_SC1>(ws, (pidx + (c8 & 3)) * 16, WS_MG + 1280 * 8); g[3] = ld4<AUX_SC1>(ws, (pidx + 4) * 16, WS_MG + 1280 * 8);
         ga = ld2<AUX_SC1>(ws, cown * 8, WS_MG); gb = ld2<AUX_SC1>(ws, cown * 8, WS_MG + 1920 * 8); cl = ld2<AUX_SC1>(ws, (lane < 20 ? lane : 0) * 128, WS_CL),
         UNR for (int i = 0; i < 4; ++i) TAG4(g[i]); TAG2(ga); TAG2(gb); TAG2(cl));
    s2[0] = asf(g[0].x); s2[1] = asf(g[0].z); s2[2] = asf(g[1].x); s2[3] = asf(g[1].z);
    c2[0] = rb(1.f + asf(g[2].x)); c2[1] = rb(1.f + asf(g[2].z)); c2[2] = rb(1.f + asf(g[3].x)); c2[3] = rb(1.f + asf(g[3].z));
    gav = asf(ga.x); gbv = asf(gb.x);
    STAMP(6);
  }
  const bool proj = !svc || wave >= 4;
  const int cg = lane & 31, half = lane >> 5;
  v4u ow[12];
  UNR for (int i = 0; i < 12; ++i) ow[i] = v4u{0u, 0u, 0u, 0u};
  const rsrc_t rout = mk(p.out, D * 2);
  float x2 = 0.f;
#pragma unroll 1
  for (int real = DRY ? 0 : 1; real < 2; ++real) {
    const int drop = real ? 0 : OOB;
    if (proj) {
      if (real) {
        if (!svc && !lds_wait_flag(LV + L_WF + w)) return;
        STAMP(7);
        const rsrc_t ro = mk(p.wo, QD * D * 2);
        UNR for (int i = 0; i < 12; ++i) ow[i] = ld4<AUX_W>(ro, cg < 20 ? half * 96 * 1280 + cg * 16 : OOB, ((h * DH + 8 * i + s) * D + 160 * w) * 2);
        SCHED();
        UNR for (int i = 0; i < 6; ++i) pair_rows(ow[2 * i], ow[2 * i + 1]);
        STAMP(8);
        if (!svc) BARRIER();
        STAMP(9);
      }
      v4u g[4];
      const int vo = r8 * OP_S + c8 * 16, vo3 = r8 < 4 ? vo : OOB;
      const u32 m3 = r8 < 4 ? ~0u : 0u;
      TPOLL(1, UNR for (int i = 0; i < 4; ++i) g[i] = ld4<AUX_SC1>(ws, i < 3 ? vo : vo3, WS_OP + (h * 32 + 8 * i) * OP_S + s * 128 + drop),
            UNR for (int i = 0; i < 4; ++i) { bad |= i < 3 ? XOR4(g[i]) : (XOR4(g[i]) & m3); KEEPV(bad); });
      STAMP(10);
      u32 pr[3];
      UNR for (int t = 0; t < 3; ++t) { const float of = sum_hi((asf(g[0][t]) + asf(g[1][t])) + (asf(g[2][t]) + asf(g[3][t])));
        pr[t] = pack2(of, dpp<XOR1>(of)); }
      const u32 pa = half ? dppu<0x124>(pr[1]) : pr[0], pb = half ? dppu<0x124>(pr[2]) : pr[0], pc = half ? dppu<0x124>(pr[2]) : pr[1];
      float acc[8] = ZERO8;
      mac8(acc, ow[0], ow[1], dppu<0x150>(pa)); mac8(acc, ow[2], ow[3], dppu<0x152>(pa));
      mac8(acc, ow[4], ow[5], dppu<0x154>(pb)); mac8(acc, ow[6], ow[7], dppu<0x156>(pb));
      mac8(acc, ow[8], ow[9], dppu<0x150>(pc)); mac8(acc, ow[10], ow[11], dppu<0x152>(pc));
      const v4u res = {canon(tr32(acc[0], acc[4])), canon(tr32(acc[1], acc[5])), canon(tr32(acc[2], acc[6])), canon(tr32(acc[3], acc[7]))};
      if (cg < 20) st4(ws, (cg >> 2) * AP_S + (cg & 3) * 32 + half * 16, WS_AP + 5 * w * AP_S + b * 128 + drop, res);
      STAMP(11);
    }
    if (mlp) {
      BARRIER();
      if (j < 20) {
        v4u g[4];
        TPOLL(0, UNR for (int i = 0; i < 4; ++i) g[i] = ld4<AUX_SC1>(ws, lane * 16 + i * 1024, WS_AP + j * AP_S + wave * 4096 + drop),
              bad = umax(umax4(umax4(0u, g[0]), g[1]), umax4(umax4(0u, g[2]), g[3])) == SENT);
        STAMP(12);
        float a[4];
        UNR for (int e = 0; e < 4; ++e) a[e] = (asf(g[0][e]) + asf(g[1][e])) + (asf(g[2][e]) + asf(g[3][e]));
        float r = tr16(tr32(a[0], a[2]), tr32(a[1], a[3])); r += dpp<ROR8>(r);
        if (!(lane & 8)) L[L_RED + (4 * c8 + (lane >> 4)) * 8 + wave] = asu(r);
        BARRIER();
        if (wave == 0) {
          const v4u t0 = *(const LAS v4u*)(L + L_RED + (lane & 31) * 8), t1 = *(const LAS v4u*)(L + L_RED + (lane & 31) * 8 + 4);
          const float at = rb(SUM8(asf(t0.x), asf(t0.y), asf(t0.z), asf(t0.w), asf(t1.x), asf(t1.y), asf(t1.z), asf(t1.w)));
          x2 = rb(xv + rb(gav * at));
          u32 xp = pack2(x2, dpp<XOR1>(x2));
          xp = xp < 0xFFFFFFFEu ? xp : 0xFFFEFFFFu;
          const u32 x1 = dppu<XOR2>(xp);
          const v4u xo = {xp, x1, dppu<0x104>(xp), dppu<0x104>(x1)};
          if (lane < 32 && c8 == 0) st4(ws, lane * 2, WS_X2 + j * 64 + drop, xo);
        }
      }
      {
        v4u xa; u32 xb, x0, x1;
        TPOLL(0, xa = ld4<AUX_SC1>(ws, lane * 16, WS_X2 + drop); xb = ld1<AUX_SC1>(ws, lane * 4 + 1024, WS_X2 + drop);
              x0 = ld1<AUX_SC1>(ws, (pidx + (c8 & 3)) * 4, WS_X2 + drop); x1 = ld1<AUX_SC1>(ws, (pidx + 4) * 4, WS_X2 + drop),
              bad = umax4(umax(umax(xb, x0), x1), xa) == SENT);
        STAMP(13);
        const u32 xw[5] = {xa.x, xa.y, xa.z, xa.w, xb};
        float mb, rden;
        ln_stats(xw, mb, rden);
        const u32 ha = ada_pair(x0, mb, rden, c2[0], c2[1], s2[0], s2[1]), h4 = ada_pair(x1, mb, rden, c2[2], c2[3], s2[2], s2[3]);
        const u32 hp[5] = {dppu<0x00>(ha), dppu<0x55>(ha), dppu<0xAA>(ha), dppu<0xFF>(ha), h4};
        float ag[8] = ZERO8, au[8] = ZERO8;
        UNR for (int i = 0; i < 5; ++i) { mac8(ag, gw[2 * i], gw[2 * i + 1], hp[i]); mac8(au, uw[2 * i], uw[2 * i + 1], hp[i]); }
        const float gs = tr_reduce8(ag, lane), us = tr_reduce8(au, lane);
        L[L_GU + (8 * c8 + r8) * 20 + wave] = asu(gs);
        L[L_GU + (8 * c8 + r8) * 20 + 8 + wave] = asu(us);
        BARRIER();
      }
      if (wave < 5) {
        const v4u t0 = *(const LAS v4u*)(L + L_GU + lane * 20), t1 = *(const LAS v4u*)(L + L_GU + lane * 20 + 4), t2 = *(const LAS v4u*)(L + L_GU + lane * 20 + 8), t3 = *(const LAS v4u*)(L + L_GU + lane * 20 + 12);
        const float gg = rb(SUM8(asf(t0.x), asf(t0.y), asf(t0.z), asf(t0.w), asf(t1.x), asf(t1.y), asf(t1.z), asf(t1.w)));
        const float uu = rb(SUM8(asf(t2.x), asf(t2.y), asf(t2.z), asf(t2.w), asf(t3.x), asf(t3.y), asf(t3.z), asf(t3.w)));
        const float hh = gg * uu;
        const u32 hq = pack2(hh, dpp<XOR1>(hh));
        float acc[8] = ZERO8;
        mac8(acc, dw[0], dw[1], dppu<0x150>(hq)); mac8(acc, dw[2], dw[3], dppu<0x152>(hq)); mac8(acc, dw[4], dw[5], dppu<0x154>(hq)); mac8(acc, dw[6], dw[7], dppu<0x156>(hq));
        mac8(acc, dw[8], dw[9], dppu<0x158>(hq)); mac8(acc, dw[10], dw[11], dppu<0x15A>(hq)); mac8(acc, dw[12], dw[13], dppu<0x15C>(hq)); mac8(acc, dw[14], dw[15], dppu<0x15E>(hq));
        float a[4];
        UNR for (int e = 0; e < 4; ++e) { a[e] = tr32(acc[e], acc[4 + e]); a[e] = tr16(a[e], a[e]); }
        if (!(lane & 16)) st4(ws, ((lane & 15) >> 2) * DP_S + (lane & 3) * 32 + half * 16, WS_DP + 4 * wave * DP_S + j * 128 + drop, v4u{canon(a[0]), canon(a[1]), canon(a[2]), canon(a[3])});
      }
      STAMP(14);
      if (j < 20 && wave == 0) {
        v4u g[3];
        TPOLL(0, UNR for (int i = 0; i < 3; ++i) g[i] = ld4<AUX_SC1>(ws, lane * 16 + i * 1024, WS_DP + j * DP_S + drop),
              bad = umax4(umax4(umax4(0u, g[0]), g[1]), g[2]) == SENT);
        float a[4];
        UNR for (int e = 0; e < 4; ++e) a[e] = (asf(g[0][e]) + asf(g[1][e])) + asf(g[2][e]);
        float r = tr16(tr32(a[0], a[2]), tr32(a[1], a[3])); r += dpp<ROR8>(r);
        const int c = lane & 31;
        const float dn = rb(asf(__builtin_amdgcn_ds_bpermute(((c >> 2) + (c & 3) * 16) * 4, asu(r))));
        const float o = x2 + rb(gbv * dn);
        if (lane < 32) __builtin_amdgcn_raw_buffer_store_b16((u16)pack2(o, o), rout, lane * 2, 64 * j + drop, 0);
        if (j == 0 && lane == 0) st1(ws, 0, WS_EPOCH + drop, T);
        STAMP(15);
      }
    }
  }
  STAMP_DUMP();
}

extern "C" void kernel_launch(void* const* d_in, const int* in_sizes, int n_in, void* d_out, int out_size, void* d_ws, size_t ws_size, hipStream_t stream) {
  Params p;
  p.x = (const u16*)d_in[0]; p.K = (const u16*)d_in[1]; p.V = (const u16*)d_in[2]; p.wq = (const u16*)d_in[3]; p.wo = (const u16*)d_in[4];
  p.wg = (const u16*)d_in[6]; p.wu = (const u16*)d_in[7]; p.wd = (const u16*)d_in[8]; p.c = (const u16*)d_in[9]; p.wmod = (const u16*)d_in[10];
  p.out = (u16*)d_out; p.ws = (unsigned char*)d_ws;
  hipLaunchKernelGGL(fused_block, dim3(NWG), dim3(NTHR), 0, stream, p);
}
```

```cpp
#include <hip/hip_runtime.h>
#include <stdint.h>
#include <stdio.h>
#pragma clang fp contract(off)

typedef unsigned short u16;
typedef unsigned u32;
typedef unsigned long long u64;
typedef u32 v4u __attribute__((ext_vector_type(4)));
typedef u32 v2u __attribute__((ext_vector_type(2)));
typedef __bf16 bf16x2 __attribute__((ext_vector_type(2)));
#define LAS __attribute__((address_space(3)))
#define GAS __attribute__((address_space(1)))
typedef __amdgpu_buffer_rsrc_t rsrc_t;

constexpr int D = 640, H = 32, SK = 4096, DH = 192, F = 1536, QD = H * DH;
constexpr int NWG = 256, NTHR = 512;
constexpr int AUX_NT = 2, AUX_SC1 = 16;
#define AUX_KV 2
#define AUX_W 2
#define WK 7
#define WV 7
#define AUX_GU 0

constexpr int WS_EPOCH = 0;
constexpr int WS_HG = 4096;
constexpr int WS_MP = WS_HG + 4096;
constexpr int MP_S = 1280 * 8 + 640, OP_S = 1024 + 128, AP_S = 32768 + 128, DP_S = 3072 + 128;
constexpr int WS_MG = WS_MP + 8 * MP_S;
constexpr int WS_QP = WS_MG + 2560 * 8;
constexpr int WS_ML = WS_QP + 32 * 8 * 1024;
constexpr int WS_OP = WS_ML + 32 * 32 * 16;
constexpr int WS_AP = WS_OP + 32 * 32 * OP_S;
constexpr int WS_DP = WS_AP + 20 * AP_S;
constexpr int WS_X2 = WS_DP + 20 * DP_S;
constexpr int WS_CL = WS_X2 + 1280;
constexpr int WS_END = WS_CL + 20 * 128;
constexpr int WS_STAMP = 8 << 20;
constexpr int RED0 = 224;
constexpr u32 SENT = 0xFFFFFFFFu;
constexpr int OOB = 0x40000000;
constexpr u32 SPIN_CAP = 1u << 20;

struct Params {
  const u16 *x, *K, *V, *wq, *wo, *wg, *wu, *wd, *c, *wmod;
  u16* out;
  unsigned char* ws;
};

__device__ __forceinline__ float asf(u32 u) { return __builtin_bit_cast(float, u); }
__device__ __forceinline__ u32 asu(float f) { return __builtin_bit_cast(u32, f); }
__device__ __forceinline__ float lo_f(u32 w) { return asf(w << 16); }
__device__ __forceinline__ float hi_f(u32 w) { return asf(w & 0xffff0000u); }
__device__ __forceinline__ u32 pack2(float lo, float hi) { bf16x2 v = {(__bf16)lo, (__bf16)hi}; return __builtin_bit_cast(u32, v); }
__device__ __forceinline__ float rb(float f) { return (float)(__bf16)f; }
__device__ __forceinline__ float dot2(u32 a, u32 b, float c) { return __builtin_amdgcn_fdot2_f32_bf16(__builtin_bit_cast(bf16x2, a), __builtin_bit_cast(bf16x2, b), c, false); }
__device__ __forceinline__ u32 perm_lo(u32 a, u32 b) { return __builtin_amdgcn_perm(b, a, 0x05040100u); }
__device__ __forceinline__ u32 perm_hi(u32 a, u32 b) { return __builtin_amdgcn_perm(b, a, 0x07060302u); }
template <int CTRL> __device__ __forceinline__ float dpp(float x) { return asf(__builtin_amdgcn_mov_dpp(asu(x), CTRL, 0xf, 0xf, true)); }
constexpr int XOR1 = 0xB1, XOR2 = 0x4E, HMIR = 0x141, ROR8 = 0x128;
__device__ __forceinline__ float tr32(float a, float b) { auto s = __builtin_amdgcn_permlane32_swap(asu(a), asu(b), false, false); return asf(s[0]) + asf(s[1]); }
__device__ __forceinline__ float tr16(float a, float b) { auto s = __builtin_amdgcn_permlane16_swap(asu(a), asu(b), false, false); return asf(s[0]) + asf(s[1]); }
__device__ __forceinline__ float tr8(float a, float b, bool bit3) { const float keep = bit3 ? b : a, send = bit3 ? a : b; return keep + dpp<ROR8>(send); }
__device__ __forceinline__ float max32(float a) { auto s = __builtin_amdgcn_permlane32_swap(asu(a), asu(a), false, false); return fmaxf(asf(s[0]), asf(s[1])); }
__device__ __forceinline__ float max16(float a) { auto s = __builtin_amdgcn_permlane16_swap(asu(a), asu(a), false, false); return fmaxf(asf(s[0]), asf(s[1])); }
__device__ __forceinline__ float sum8(float a) { a += dpp<XOR1>(a); a += dpp<XOR2>(a); a += dpp<HMIR>(a); return a; }
__device__ __forceinline__ float sum_hi(float a) { a += dpp<ROR8>(a); a = tr16(a, a); return tr32(a, a); }
__device__ __forceinline__ float wave_sum(float a) { return sum_hi(sum8(a)); }
__device__ __forceinline__ float max_hi(float a) { a = fmaxf(a, dpp<ROR8>(a)); return max32(max16(a)); }
__device__ __forceinline__ float wave_max(float a) { a = fmaxf(a, dpp<XOR1>(a)); a = fmaxf(a, dpp<XOR2>(a)); a = fmaxf(a, dpp<HMIR>(a)); return max_hi(a); }
__device__ __forceinline__ float tr_reduce8(const float* v, int lane) {
  const float a0 = tr32(v[0], v[4]), a1 = tr32(v[1], v[5]), a2 = tr32(v[2], v[6]), a3 = tr32(v[3], v[7]);
  const float b0 = tr16(a0, a2), b1 = tr16(a1, a3);
  return tr8(b0, b1, (lane & 8) != 0);
}
__device__ __forceinline__ rsrc_t mk(const void* p, int bytes) { return __builtin_amdgcn_make_buffer_rsrc((void*)p, 0, bytes, 0x00020000); }
template <int AUX> __device__ __forceinline__ v4u ld4(rsrc_t r, int vo, int so) { return __builtin_amdgcn_raw_buffer_load_b128(r, vo, so, AUX); }
template <int AUX> __device__ __forceinline__ v2u ld2(rsrc_t r, int vo, int so) { return __builtin_amdgcn_raw_buffer_load_b64(r, vo, so, AUX); }
template <int AUX> __device__ __forceinline__ u32 ld1(rsrc_t r, int vo, int so) { return __builtin_amdgcn_raw_buffer_load_b32(r, vo, so, AUX); }
__device__ __forceinline__ void st4(rsrc_t r, int vo, int so, v4u v) { __builtin_amdgcn_raw_buffer_store_b128(v, r, vo, so, AUX_SC1); }
__device__ __forceinline__ void st2(rsrc_t r, int vo, int so, v2u v) { __builtin_amdgcn_raw_buffer_store_b64(v, r, vo, so, AUX_SC1); }
__device__ __forceinline__ void st1(rsrc_t r, int vo, int so, u32 v) { __builtin_amdgcn_raw_buffer_store_b32(v, r, vo, so, AUX_SC1); }
__device__ __forceinline__ u32 umax(u32 a, u32 b) { return a > b ? a : b; }
__device__ __forceinline__ u32 canon(float f) { const u32 u = asu(f); return u < 0xFFFFFFFEu ? u : 0xFFFFFFFEu; }
template <int CTRL> __device__ __forceinline__ u32 dppu(u32 x) { return __builtin_amdgcn_mov_dpp(x, CTRL, 0xf, 0xf, true); }
#define CFENCE() asm volatile("" ::: "memory")
#define LDS_WAIT() asm volatile("s_waitcnt lgkmcnt(0)" ::: "memory")
#define BARRIER() asm volatile("s_waitcnt lgkmcnt(0)\n\ts_barrier" ::: "memory")
#define SCHED() __builtin_amdgcn_sched_barrier(0)
#define KEEPV(x) asm volatile("" : "+v"(x))
__device__ __forceinline__ float frcp(float x) { return __builtin_amdgcn_rcpf(x); }
__device__ __forceinline__ float fsqrt(float x) { return __builtin_amdgcn_sqrtf(x); }
__device__ __forceinline__ float silu(float x) { return x / (1.f + expf(-x)); }
template <int N> __device__ __forceinline__ void vm_wait() { asm volatile("s_waitcnt vmcnt(%0)" :: "i"(N) : "memory"); }
__device__ __forceinline__ bool all_zero(u32 bad) { return __builtin_amdgcn_ballot_w64(bad != 0u) == 0ull; }
__device__ __forceinline__ bool lds_wait_flag(volatile LAS u32* f) {
#pragma unroll 1
  for (u32 sp = 0; sp < (SPIN_CAP << 3); ++sp) {
    if (__builtin_amdgcn_readfirstlane(*f) != 0u) { CFENCE(); return true; }
    __builtin_amdgcn_s_sleep(1);
  }
  return false;
}
#define STAMP(i) do {} while (0)
#define STAMP_DUMP() do {} while (0)
#define STAMP_DECL()

constexpr int L_KF = 0, L_GF = 4, L_PF = 8, L_QF = 12, L_T = 13, L_TF = 14, L_CNT = 15, L_WF = 16, L_HS = 24  , L_QS = 64  ,
              L_PS = 160  , L_SI = 544  , L_RED = 864  , L_GU = 1888  ,
              L_SPILL = 3168  , L_WORDS = L_SPILL + 8 * 9 * 256;
constexpr float LOG2E = 1.4426950408889634f;
constexpr int NG = 19, NL = 3 * NG;
#define GSTEP 36
#define WOSTEP 44
#define HSLEEP 2
#define WK0 3
#define WQW 16
#define WS_ 4
#define DRY 0

#define POLL(SLEEP, LOADS, CHECK) do { u32 sp_ = 0; for (;; ++sp_) { CFENCE(); u32 bad = 0; LOADS; CHECK; if (all_zero(bad)) break; if (sp_ > SPIN_CAP) return; \
    if (SLEEP) __builtin_amdgcn_s_sleep(SLEEP); } } while (0)
#define TPOLL(SLEEP, LOADS, CHECK) do { u32 sp_ = 0; for (;; ++sp_) { CFENCE(); u32 bad = 0; LOADS; CHECK; if (all_zero(bad) || !real) break; if (sp_ > SPIN_CAP) return; \
    if (SLEEP) __builtin_amdgcn_s_sleep(SLEEP); } } while (0)
#define TAG2(v) do { bad |= (v).y ^ T; KEEPV(bad); } while (0)
#define TAG4(v) do { bad |= ((v).y ^ T) | ((v).w ^ T); KEEPV(bad); } while (0)
#define XOR4(v) ((v).x ^ (v).y ^ (v).z ^ (v).w ^ T)
#define UNR _Pragma("unroll")
#define SUM8(a0, a1, a2, a3, a4, a5, a6, a7) ((((a0) + (a1)) + ((a2) + (a3))) + (((a4) + (a5)) + ((a6) + (a7))))
#define ZERO8 {0.f, 0.f, 0.f, 0.f, 0.f, 0.f, 0.f, 0.f}

__device__ __forceinline__ u32 ada_pair(u32 xp, float mb, float rden, float c0, float c1, float s0, float s1) {
  return pack2(rb(rb(rb(lo_f(xp) - mb) * rden) * c0) + s0, rb(rb(rb(hi_f(xp) - mb) * rden) * c1) + s1);
}
__device__ __forceinline__ void ln_stats(const u32* xw, float& mb, float& rden) {
  const float sm = (((lo_f(xw[0]) + hi_f(xw[0])) + (lo_f(xw[1]) + hi_f(xw[1]))) + ((lo_f(xw[2]) + hi_f(xw[2])) + (lo_f(xw[3]) + hi_f(xw[3])))) + (lo_f(xw[4]) + hi_f(xw[4]));
  const float mean = wave_sum(sm) * (1.f / D);
  float sq0 = 0.f, sq1 = 0.f;
  UNR for (int i = 0; i < 5; ++i) { const float d0 = lo_f(xw[i]) - mean, d1 = hi_f(xw[i]) - mean; sq0 = fmaf(d0, d0, sq0); sq1 = fmaf(d1, d1, sq1); }
  const float var = rb(wave_sum(sq0 + sq1) * (1.f / D));
  rden = frcp(rb(fsqrt(rb(var + 1e-5f))));
  mb = rb(mean);
}
__device__ __forceinline__ void pair_rows(v4u& a, v4u& c) { UNR for (int e = 0; e < 4; ++e) { const u32 x = a[e], y = c[e]; a[e] = perm_lo(x, y); c[e] = perm_hi(x, y); } KEEPV(a); KEEPV(c); }
__device__ __forceinline__ void mac8(float* acc, const v4u& lo, const v4u& hi, u32 ap) { UNR for (int e = 0; e < 4; ++e) { acc[2 * e] = dot2(lo[e], ap, acc[2 * e]); acc[2 * e + 1] = dot2(hi[e], ap, acc[2 * e + 1]); } }
__device__ __forceinline__ u32 umax4(u32 m, const v4u& g) { return umax(umax(umax(m, g.x), g.y), umax(g.z, g.w)); }

__global__ void __launch_bounds__(NTHR, 2) fused_block(Params p) {
  __shared__ __attribute__((aligned(16))) u32 smem[L_WORDS];
  LAS u32* L = (LAS u32*)smem;
  volatile LAS u32* LV = (volatile LAS u32*)smem;
  const int tid = threadIdx.x, lane = tid & 63, wave = __builtin_amdgcn_readfirstlane(tid >> 6), b = blockIdx.x;
  const int h = (b & 7) * 4 + ((b >> 3) & 3), s = b >> 5, r8 = lane >> 3, c8 = lane & 7, w = wave & 3;
  const bool svc = s == 7, mlp = svc && (h & 3) != 3;
  const int j = 3 * (h >> 2) + (h & 3), xi = h >> 2;
  const rsrc_t ws = mk(p.ws, WS_END);
  const int l40 = lane < 40 ? lane : 39;
  u32 T = 0;
  STAMP_DECL();
  STAMP(0);
  const rsrc_t rm = mk(p.wmod, D * 6 * D * 2), rc = mk(p.c, D * 2);
#define MKTAG(e) (__builtin_amdgcn_readfirstlane(e) * 0x9E3779B1u + 0x7F4A7C15u)
#define MOD_LD(i, rowblk, colbyte) ld4<AUX_W>(rm, r8 * 2 * 7680 + c8 * 16, (80 * (rowblk) + 16 * ((i) >> 1) + ((i) & 1)) * 7680 + (colbyte))
#define MOD_SILU(cw, sp) do { L[L_SI + wave * 40 + l40] = pack2(silu(lo_f(cw)), silu(hi_f(cw))); LDS_WAIT(); UNR for (int i = 0; i < 5; ++i) sp[i] = L[L_SI + wave * 40 + 8 * i + r8]; } while (0)
#define MOD_MAC(res, m, sp) do { float acc_[8] = ZERO8; \
    UNR for (int i = 0; i < 5; ++i) { pair_rows(m[2 * i], m[2 * i + 1]); mac8(acc_, m[2 * i], m[2 * i + 1], sp[i]); } res = tr_reduce8(acc_, lane); } while (0)
  if (wave == 5 && b < 160) {
    __builtin_amdgcn_s_setprio(3);
    const int blk = b >> 3, rblk = b & 7;
    u32 sp[5]; v4u m0[10];
    const u32 cw = ld1<0>(rc, l40 * 4, rblk * 160);
    UNR for (int i = 0; i < 10; ++i) m0[i] = MOD_LD(i, rblk, 128 * blk);
    const u32 E = ld1<AUX_SC1>(ws, 0, WS_EPOCH);
    SCHED();
    BARRIER();
    MOD_SILU(cw, sp);
    float v;
    MOD_MAC(v, m0, sp);
    const u32 Tq = MKTAG(E), vn = dppu<ROR8>(asu(v));
    if (!(lane & 8)) st4(ws, (8 * c8 + r8) * 8, WS_MP + rblk * MP_S + 512 * blk, v4u{asu(v), Tq, vn, Tq});
    __builtin_amdgcn_s_setprio(0);
    STAMP(1);
  } else if (wave == 5 && b < 180) {
    BARRIER();
    const int jc = b - 160;
    const u32 E = ld1<AUX_SC1>(ws, 0, WS_EPOCH);
    const v4u sv = {SENT, SENT, SENT, SENT};
    UNR for (int i = 0; i < 32; ++i) { if (i >= 8) vm_wait<7>(); st4(ws, lane * 16, WS_AP + jc * AP_S + i * 1024, sv); SCHED(); }
    UNR for (int i = 0; i < 3; ++i) st4(ws, lane * 16, WS_DP + jc * DP_S + i * 1024, sv);
    if (lane < 4) st4(ws, lane * 16, WS_X2 + jc * 64, sv);
    const u32 Tq = MKTAG(E);
    vm_wait<0>();
    if (lane == 0) st4(ws, 0, WS_CL + jc * 128, v4u{0u, Tq, 0u, Tq});
    STAMP(2);
  } else if (!svc && (wave == 4 || (wave == 6 && s == 6))) {
    __builtin_amdgcn_s_setprio(3);
    const int sl = wave == 4 ? s : 7;
    LAS u32* hs = L + (wave == 4 ? L_HS : L_RED);
    BARRIER();
    const rsrc_t rqw = mk(p.wq, D * QD * 2);
    v4u qw[30];
    UNR for (int i = 0; i < 30; ++i) {
      if (i >= WQW) vm_wait<WQW - 1>();
      qw[i] = ld4<AUX_W>(rqw, r8 * 2 * 12288 + c8 * 16 + (i / 10) * 128, (80 * sl + 16 * ((i % 10) >> 1) + (i & 1)) * 12288 + h * DH * 2);
      SCHED();
    }
    const u32 E = ld1<AUX_SC1>(ws, 0, WS_EPOCH);
    SCHED();
    UNR for (int i = 0; i < 15; ++i) pair_rows(qw[2 * i], qw[2 * i + 1]);
    T = MKTAG(E);
    if (wave == 4) { L[L_T] = T; LDS_WAIT(); LV[L_TF] = 1u; }
    STAMP(3);
    {
      v2u g;
      POLL(HSLEEP, g = ld2<AUX_SC1>(ws, l40 * 8, WS_HG + sl * 320), TAG2(g));
      hs[l40] = g.x;
      LDS_WAIT();
    }
    STAMP(4);
    {
      u32 hp[5];
      UNR for (int i = 0; i < 5; ++i) hp[i] = hs[8 * i + r8];
      u32 qv[3];
      UNR for (int t = 0; t < 3; ++t) {
        float acc[8] = ZERO8;
        UNR for (int i = 0; i < 5; ++i) mac8(acc, qw[10 * t + 2 * i], qw[10 * t + 2 * i + 1], hp[i]);
        qv[t] = asu(tr_reduce8(acc, lane));
      }
      st4(ws, lane * 16, WS_QP + (h * 8 + sl) * 1024, v4u{qv[0], qv[1], qv[2], qv[0] ^ qv[1] ^ qv[2] ^ T});
    }
    __builtin_amdgcn_s_setprio(0);
    STAMP(5);
  } else if (svc && !mlp && wave == 0) {
    const rsrc_t rx = mk(p.x, D * 2);
    u32 xw[5];
    UNR for (int i = 0; i < 5; ++i) xw[i] = ld1<0>(rx, lane * 4 + i * 256, 0);
    const u32 xme = ld1<0>(rx, l40 * 4, xi * 160);
    const u32 E = ld1<AUX_SC1>(ws, 0, WS_EPOCH);
    SCHED();
    BARRIER();
    float mb, rden;
    ln_stats(xw, mb, rden);
    T = MKTAG(E);
    v4u g[16];
    POLL(0, UNR for (int q = 0; q < 16; ++q) g[q] = ld4<AUX_SC1>(ws, l40 * 16, WS_MP + (q & 7) * MP_S + ((q >> 3) * 640 + 80 * xi) * 8),
         UNR for (int q = 0; q < 16; ++q) TAG4(g[q]));
    float v[4];
    UNR for (int k = 0; k < 4; ++k) { const int o = 8 * (k >> 1), e = 2 * (k & 1);
      v[k] = rb(SUM8(asf(g[o][e]), asf(g[o + 1][e]), asf(g[o + 2][e]), asf(g[o + 3][e]), asf(g[o + 4][e]), asf(g[o + 5][e]), asf(g[o + 6][e]), asf(g[o + 7][e]))); }
    const u32 hp = ada_pair(xme, mb, rden, rb(1.f + v[2]), rb(1.f + v[3]), v[0], v[1]), hn = dppu<XOR1>(hp);
    if (lane < 40 && !(lane & 1)) st4(ws, lane * 8, WS_HG + xi * 320, v4u{hp, T, hn, T});
    LV[L_QF] = 1u;
    STAMP(1);
  } else {
    if (wave == 7 && lane < 24) L[lane] = 0u;
    BARRIER();
  }
  const int wid = (3 - w) * 7 + s;
  const int kvoff = r8 * 384 + c8 * 16, kvbase = (h * 512 + wid) * 3072;
  const int lastbase = wid < 8 ? kvbase + 18 * 28 * 3072 : OOB;
  LAS v4u* spill = (LAS v4u*)(L + L_SPILL + wave * 9 * 256) + lane;
  if (!svc && wave >= 4) {
    if (!lds_wait_flag(LV + L_KF + w)) return;
    STAMP(7);
    SCHED();
    const rsrc_t rv = mk(p.V, H * SK * DH * 2);
    v4u vr[48];
#define VR(i) vr[(i) % 48]
#define VISSUE(i) do { if ((i) >= WV) vm_wait<WV - 1>(); \
      VR(i) = (i) < 3 ? ld4<AUX_KV>(rv, kvoff + (i) * 128, lastbase) \
                      : ld4<AUX_KV>(rv, kvoff + ((((i) + 3) % 6) >> 1) * 128, kvbase + (2 * (((i) - 3) / 6) + (((i) + 1) & 1)) * (28 * 3072)); SCHED(); \
      if ((i) - WV >= 4 && !(((i) - WV) & 1)) pair_rows(VR((i) - WV - 1), VR((i) - WV)); \
      if ((i) == 8 + WV) { UNR for (int k = 0; k < 9; ++k) spill[64 * k] = VR(k); } \
      if ((i) == GSTEP) LV[L_GF + w] = 1u; \
      if ((i) == WOSTEP) LV[L_WF + w] = 1u; SCHED(); } while (0)
    UNR for (int i = 0; i < 30; ++i) VISSUE(i);
    UNR for (int i = 30; i < NL; ++i) VISSUE(i);
    STAMP(8);
    if (!lds_wait_flag(LV + L_PF + w)) return;
    STAMP(9);
    u32 pp[12];
    UNR for (int k = 0; k < 3; ++k) { const v4u t4 = *(const LAS v4u*)(L + L_PS + (w * 8 + r8) * 12 + 4 * k); UNR for (int e = 0; e < 4; ++e) pp[4 * k + e] = t4[e]; }
    T = __builtin_amdgcn_readfirstlane(L[L_T]);
    float acc[24];
    UNR for (int i = 0; i < 24; ++i) acc[i] = 0.f;
    constexpr int LASTP = ((NL - 1 - WV) & ~1);
#define PV_T(i, t) do { if (4 + 6 * (i) + 2 * (t) > LASTP) pair_rows(VR(3 + 6 * (i) + 2 * (t)), VR(4 + 6 * (i) + 2 * (t))); \
      mac8(acc + 8 * (t), VR(3 + 6 * (i) + 2 * (t)), VR(4 + 6 * (i) + 2 * (t)), pp[i]); } while (0)
#define PV(i) do { PV_T(i, 0); PV_T(i, 1); PV_T(i, 2); SCHED(); } while (0)
    PV(1); PV(2);
    {
      v4u sv[9];
      UNR for (int k = 0; k < 9; ++k) sv[k] = spill[64 * k];
      SCHED();
      PV(3); PV(4);
      UNR for (int t = 0; t < 3; ++t) {
        UNR for (int e = 0; e < 4; ++e) { acc[8 * t + 2 * e] = dot2(sv[t][e] & 0xffffu, pp[9], acc[8 * t + 2 * e]); acc[8 * t + 2 * e + 1] = dot2(sv[t][e] & 0xffff0000u, pp[10], acc[8 * t + 2 * e + 1]); }
        mac8(acc + 8 * t, sv[3 + 2 * t], sv[4 + 2 * t], pp[0]);
      }
      SCHED();
    }
    PV(5); PV(6);
    STAMP(10);
    PV(7);
    PV_T(8, 0);
    const float o0 = tr_reduce8(acc, lane);
    SCHED();
    PV_T(8, 1);
    const float o1 = tr_reduce8(acc + 8, lane);
    SCHED();
    PV_T(8, 2);
    const float o2 = tr_reduce8(acc + 16, lane);
    st4(ws, lane * 16, WS_OP + (h * 32 + wid) * OP_S, v4u{asu(o0), asu(o1), asu(o2), asu(o0) ^ asu(o1) ^ asu(o2) ^ T});
    STAMP(11);
    STAMP_DUMP();
    return;
  }
  v4u gw[10], uw[10], dw[16];
  float c2[4], s2[4], gav = 0.f, gbv = 0.f, xv = 0.f;
  const int pidx = 40 * wave + 5 * r8;
  if (!svc) {
    const rsrc_t rk = mk(p.K, H * SK * DH * 2);
    v4u kr[48];
#define KR(i) kr[(i) % 48]
#define KISSUE(i) do { if ((i) >= 8) vm_wait<WK - 1>(); else if ((i) >= WK0) vm_wait<WK0 - 1>(); \
      KR(i) = ld4<AUX_KV>(rk, kvoff + ((i) % 3) * 128, (i) < 54 ? kvbase + ((i) / 3) * (28 * 3072) : lastbase); SCHED(); \
      if ((i) == 8 + WK) { UNR for (int k = 0; k < 9; ++k) spill[64 * k] = KR(k); SCHED(); } } while (0)
    UNR for (int i = 0; i < NL; ++i) KISSUE(i);
    LV[L_KF + w] = 1u;
    STAMP(1);
    if (!lds_wait_flag(LV + L_TF)) return;
    T = __builtin_amdgcn_readfirstlane(L[L_T]);
    if (w == 0) {
      v4u g[8];
      POLL(4, UNR for (int q = 0; q < 8; ++q) g[q] = ld4<AUX_SC1>(ws, lane * 16, WS_QP + (h * 8 + q) * 1024),
           UNR for (int i = 0; i < 8; ++i) { bad |= XOR4(g[i]); KEEPV(bad); });
      LAS u16* qs = (LAS u16*)(L + L_QS);
      UNR for (int t = 0; t < 3; ++t) {
        const float q = SUM8(asf(g[0][t]), asf(g[1][t]), asf(g[2][t]), asf(g[3][t]), asf(g[4][t]), asf(g[5][t]), asf(g[6][t]), asf(g[7][t]));
        qs[64 * t + 8 * c8 + r8] = (u16)pack2(q, q);
      }
      LDS_WAIT();
      LV[L_QF] = 1u;
    } else if (!lds_wait_flag(LV + L_QF)) return;
    CFENCE();
    STAMP(2);
    v4u qp[3];
    float sc[NG];
    UNR for (int t = 0; t < 3; ++t) qp[t] = *(const LAS v4u*)(L + L_QS + 32 * t + 4 * c8);
#define QK(g, k0, k1, k2) do { float a_ = 0.f; UNR for (int e = 0; e < 4; ++e) a_ = dot2((k0)[e], qp[0][e], a_); UNR for (int e = 0; e < 4; ++e) a_ = dot2((k1)[e], qp[1][e], a_); \
      UNR for (int e = 0; e < 4; ++e) a_ = dot2((k2)[e], qp[2][e], a_); sc[g] = rb(rb(sum8(a_)) * 0.07216878364870322f); } while (0)
    UNR for (int g = 3; g < NG; ++g) QK(g, KR(3 * g), KR(3 * g + 1), KR(3 * g + 2));
    SCHED();
    UNR for (int g = 0; g < 3; ++g) { const v4u k0 = spill[64 * (3 * g)], k1 = spill[64 * (3 * g + 1)], k2 = spill[64 * (3 * g + 2)]; QK(g, k0, k1, k2); }
    if (wid >= 8) sc[18] = -INFINITY;
    float m = sc[0];
    UNR for (int g = 1; g < NG; ++g) m = fmaxf(m, sc[g]);
    m = max_hi(m);
    float l = 0.f;
    UNR for (int g = 0; g < NG; ++g) l += __builtin_amdgcn_exp2f((sc[g] - m) * LOG2E);
    l = sum_hi(l);
    if (lane == 0) st4(ws, 0, WS_ML + (h * 32 + wid) * 16, v4u{asu(m), T, asu(l), T});
    STAMP(4);
    if (!lds_wait_flag(LV + L_GF + w)) return;
    STAMP(5);
    float M, Ls;
    {
      const int l32 = lane & 31;
      const int vo = lane < 32 ? WS_ML + h * 512 + (l32 < 28 ? l32 : 0) * 16 : WS_CL + (l32 < 20 ? l32 : 0) * 128;
      v4u g;
      POLL(8, g = ld4<AUX_SC1>(ws, vo, 0), TAG4(g));
      const float mm = lane < 28 ? asf(g.x) : -INFINITY;
      M = wave_max(mm);
      Ls = wave_sum(lane < 28 ? asf(g.z) * __builtin_amdgcn_exp2f((mm - M) * LOG2E) : 0.f);
    }
    {
      const float inv = 1.f / Ls;
      v4u pp[3];
      UNR for (int i = 0; i < 9; ++i)
        pp[i >> 2][i & 3] = pack2(__builtin_amdgcn_exp2f((sc[2 * i] - M) * LOG2E) * inv, __builtin_amdgcn_exp2f((sc[2 * i + 1] - M) * LOG2E) * inv);
      const float pl = __builtin_amdgcn_exp2f((sc[18] - M) * LOG2E) * inv;
      pp[2][1] = pack2(pl, 0.f); pp[2][2] = pack2(0.f, pl); pp[2][3] = 0u;
      if (c8 == 0) { UNR for (int k = 0; k < 3; ++k) *(LAS v4u*)(L + L_PS + (w * 8 + r8) * 12 + 4 * k) = pp[k]; }
      LDS_WAIT();
      LV[L_PF + w] = 1u;
    }
    STAMP(6);
  } else {
    if (!mlp && !lds_wait_flag(LV + L_QF)) return;
    if (mlp && j < 20) xv = lo_f(p.x[32 * j + (lane & 31)]);
    u32 sp[5]; v4u m0[10];
    const u32 cw = ld1<0>(rc, l40 * 4, wave * 160);
    const u32 E = ld1<AUX_SC1>(ws, 0, WS_EPOCH);
    UNR for (int i = 0; i < 10; ++i) m0[i] = MOD_LD(i, wave, 2560 + 128 * h);
    SCHED();
    if (!mlp) {
      UNR for (int i = 0; i < 10; ++i) gw[i] = MOD_LD(i, wave, 2560 + 128 * (32 + xi));
    } else {
      const rsrc_t rg = mk(p.wg, D * F * 2), ru = mk(p.wu, D * F * 2), rd = mk(p.wd, wave < 5 ? F * D * 2 : 0);
      UNR for (int i = 0; i < 10; ++i) { gw[i] = ld4<AUX_W>(rg, r8 * 10 * 3072 + c8 * 16, (80 * wave + i) * 3072 + 128 * j); uw[i] = ld4<AUX_W>(ru, r8 * 10 * 3072 + c8 * 16, (80 * wave + i) * 3072 + 128 * j); }
      UNR for (int i = 0; i < 16; ++i) dw[i] = ld4<AUX_W>(rd, (lane >> 4) * 16 * 1280 + (lane & 15) * 16, (64 * j + i) * 1280 + 256 * wave);
    }
    SCHED();
    MOD_SILU(cw, sp);
    T = MKTAG(E);
    float v0;
    MOD_MAC(v0, m0, sp);
    L[L_RED + wave * 64 + 8 * c8 + r8] = asu(v0);
    if (!mlp) { MOD_MAC(v0, gw, sp); L[L_RED + 512 + wave * 64 + 8 * c8 + r8] = asu(v0); }
    BARRIER();
    if (wave == 7 || (wave == 6 && !mlp)) {
      const LAS u32* a = L + L_RED + (7 - wave) * 512 + lane;
      const u32 v = asu(rb(SUM8(asf(a[0]), asf(a[64]), asf(a[128]), asf(a[192]), asf(a[256]), asf(a[320]), asf(a[384]), asf(a[448])))), vn = dppu<XOR1>(v);
      if (!(lane & 1)) st4(ws, lane * 8, WS_MG + (wave == 6 ? 32 + xi : h) * 512, v4u{v, T, vn, T});
    }
    STAMP(2);
    if (mlp) {
      UNR for (int i = 0; i < 5; ++i) { pair_rows(gw[2 * i], gw[2 * i + 1]); pair_rows(uw[2 * i], uw[2 * i + 1]); }
      UNR for (int i = 0; i < 8; ++i) pair_rows(dw[2 * i], dw[2 * i + 1]);
    }
    STAMP(3);
    v4u g[4]; v2u ga, gb, cl;
    const int cown = (j < 20 ? 32 * j : 0) + (lane & 31);
    POLL(16, g[0] = ld4<AUX_SC1>(ws, (pidx + (c8 & 3)) * 16, WS_MG + 640 * 8); g[1] = ld4<AUX_SC1>(ws, (pidx + 4) * 16, WS_MG + 640 * 8);
         g[2] = ld4<AUX_SC1>(ws, (pidx + (c8 & 3)) * 16, WS_MG + 1280 * 8); g[3] = ld4<AUX_SC1>(ws, (pidx + 4) * 16, WS_MG + 1280 * 8);
         ga = ld2<AUX_SC1>(ws, cown * 8, WS_MG); gb = ld2<AUX_SC1>(ws, cown * 8, WS_MG + 1920 * 8); cl = ld2<AUX_SC1>(ws, (lane < 20 ? lane : 0) * 128, WS_CL),
         UNR for (int i = 0; i < 4; ++i) TAG4(g[i]); TAG2(ga); TAG2(gb); TAG2(cl));
    s2[0] = asf(g[0].x); s2[1] = asf(g[0].z); s2[2] = asf(g[1].x); s2[3] = asf(g[1].z);
    c2[0] = rb(1.f + asf(g[2].x)); c2[1] = rb(1.f + asf(g[2].z)); c2[2] = rb(1.f + asf(g[3].x)); c2[3] = rb(1.f + asf(g[3].z));
    gav = asf(ga.x); gbv = asf(gb.x);
    STAMP(6);
  }
  const bool proj = !svc || wave >= 4;
  const int cg = lane & 31, half = lane >> 5;
  v4u ow[12];
  UNR for (int i = 0; i < 12; ++i) ow[i] = v4u{0u, 0u, 0u, 0u};
  const rsrc_t rout = mk(p.out, D * 2);
  float x2 = 0.f;
#pragma unroll 1
  for (int real = DRY ? 0 : 1; real < 2; ++real) {
    const int drop = real ? 0 : OOB;
    if (proj) {
      if (real) {
        if (!svc && !lds_wait_flag(LV + L_WF + w)) return;
        STAMP(7);
        const rsrc_t ro = mk(p.wo, QD * D * 2);
        UNR for (int i = 0; i < 12; ++i) ow[i] = ld4<AUX_W>(ro, cg < 20 ? half * 96 * 1280 + cg * 16 : OOB, ((h * DH + 8 * i + s) * D + 160 * w) * 2);
        SCHED();
        UNR for (int i = 0; i < 6; ++i) pair_rows(ow[2 * i], ow[2 * i + 1]);
        STAMP(8);
        if (!svc) BARRIER();
        STAMP(9);
      }
      v4u g[4];
      const int vo = r8 * OP_S + c8 * 16, vo3 = r8 < 4 ? vo : OOB;
      const u32 m3 = r8 < 4 ? ~0u : 0u;
      TPOLL(1, UNR for (int i = 0; i < 4; ++i) g[i] = ld4<AUX_SC1>(ws, i < 3 ? vo : vo3, WS_OP + (h * 32 + 8 * i) * OP_S + s * 128 + drop),
            UNR for (int i = 0; i < 4; ++i) { bad |= i < 3 ? XOR4(g[i]) : (XOR4(g[i]) & m3); KEEPV(bad); });
      STAMP(10);
      u32 pr[3];
      UNR for (int t = 0; t < 3; ++t) { const float of = sum_hi((asf(g[0][t]) + asf(g[1][t])) + (asf(g[2][t]) + asf(g[3][t])));
        pr[t] = pack2(of, dpp<XOR1>(of)); }
      const u32 pa = half ? dppu<0x124>(pr[1]) : pr[0], pb = half ? dppu<0x124>(pr[2]) : pr[0], pc = half ? dppu<0x124>(pr[2]) : pr[1];
      float acc[8] = ZERO8;
      mac8(acc, ow[0], ow[1], dppu<0x150>(pa)); mac8(acc, ow[2], ow[3], dppu<0x152>(pa));
      mac8(acc, ow[4], ow[5], dppu<0x154>(pb)); mac8(acc, ow[6], ow[7], dppu<0x156>(pb));
      mac8(acc, ow[8], ow[9], dppu<0x150>(pc)); mac8(acc, ow[10], ow[11], dppu<0x152>(pc));
      const v4u res = {canon(tr32(acc[0], acc[4])), canon(tr32(acc[1], acc[5])), canon(tr32(acc[2], acc[6])), canon(tr32(acc[3], acc[7]))};
      if (cg < 20) st4(ws, (cg >> 2) * AP_S + (cg & 3) * 32 + half * 16, WS_AP + 5 * w * AP_S + b * 128 + drop, res);
      STAMP(11);
    }
    if (mlp) {
      BARRIER();
      if (j < 20) {
        v4u g[4];
        TPOLL(0, UNR for (int i = 0; i < 4; ++i) g[i] = ld4<AUX_SC1>(ws, lane * 16 + i * 1024, WS_AP + j * AP_S + wave * 4096 + drop),
              bad = umax(umax4(umax4(0u, g[0]), g[1]), umax4(umax4(0u, g[2]), g[3])) == SENT);
        STAMP(12);
        float a[4];
        UNR for (int e = 0; e < 4; ++e) a[e] = (asf(g[0][e]) + asf(g[1][e])) + (asf(g[2][e]) + asf(g[3][e]));
        float r = tr16(tr32(a[0], a[2]), tr32(a[1], a[3])); r += dpp<ROR8>(r);
        if (!(lane & 8)) L[L_RED + (4 * c8 + (lane >> 4)) * 8 + wave] = asu(r);
        BARRIER();
        if (wave == 0) {
          const v4u t0 = *(const LAS v4u*)(L + L_RED + (lane & 31) * 8), t1 = *(const LAS v4u*)(L + L_RED + (lane & 31) * 8 + 4);
          const float at = rb(SUM8(asf(t0.x), asf(t0.y), asf(t0.z), asf(t0.w), asf(t1.x), asf(t1.y), asf(t1.z), asf(t1.w)));
          x2 = rb(xv + rb(gav * at));
          u32 xp = pack2(x2, dpp<XOR1>(x2));
          xp = xp < 0xFFFFFFFEu ? xp : 0xFFFEFFFFu;
          const u32 x1 = dppu<XOR2>(xp);
          const v4u xo = {xp, x1, dppu<0x104>(xp), dppu<0x104>(x1)};
          if (lane < 32 && c8 == 0) st4(ws, lane * 2, WS_X2 + j * 64 + drop, xo);
        }
      }
      {
        v4u xa; u32 xb, x0, x1;
        TPOLL(0, xa = ld4<AUX_SC1>(ws, lane * 16, WS_X2 + drop); xb = ld1<AUX_SC1>(ws, lane * 4 + 1024, WS_X2 + drop);
              x0 = ld1<AUX_SC1>(ws, (pidx + (c8 & 3)) * 4, WS_X2 + drop); x1 = ld1<AUX_SC1>(ws, (pidx + 4) * 4, WS_X2 + drop),
              bad = umax4(umax(umax(xb, x0), x1), xa) == SENT);
        STAMP(13);
        const u32 xw[5] = {xa.x, xa.y, xa.z, xa.w, xb};
        float mb, rden;
        ln_stats(xw, mb, rden);
        const u32 ha = ada_pair(x0, mb, rden, c2[0], c2[1], s2[0], s2[1]), h4 = ada_pair(x1, mb, rden, c2[2], c2[3], s2[2], s2[3]);
        const u32 hp[5] = {dppu<0x00>(ha), dppu<0x55>(ha), dppu<0xAA>(ha), dppu<0xFF>(ha), h4};
        float ag[8] = ZERO8, au[8] = ZERO8;
        UNR for (int i = 0; i < 5; ++i) { mac8(ag, gw[2 * i], gw[2 * i + 1], hp[i]); mac8(au, uw[2 * i], uw[2 * i + 1], hp[i]); }
        const float gs = tr_reduce8(ag, lane), us = tr_reduce8(au, lane);
        L[L_GU + (8 * c8 + r8) * 20 + wave] = asu(gs);
        L[L_GU + (8 * c8 + r8) * 20 + 8 + wave] = asu(us);
        BARRIER();
      }
      if (wave < 5) {
        const v4u t0 = *(const LAS v4u*)(L + L_GU + lane * 20), t1 = *(const LAS v4u*)(L + L_GU + lane * 20 + 4), t2 = *(const LAS v4u*)(L + L_GU + lane * 20 + 8), t3 = *(const LAS v4u*)(L + L_GU + lane * 20 + 12);
        const float gg = rb(SUM8(asf(t0.x), asf(t0.y), asf(t0.z), asf(t0.w), asf(t1.x), asf(t1.y), asf(t1.z), asf(t1.w)));
        const float uu = rb(SUM8(asf(t2.x), asf(t2.y), asf(t2.z), asf(t2.w), asf(t3.x), asf(t3.y), asf(t3.z), asf(t3.w)));
        const float hh = gg * uu;
        const u32 hq = pack2(hh, dpp<XOR1>(hh));
        float acc[8] = ZERO8;
        mac8(acc, dw[0], dw[1], dppu<0x150>(hq)); mac8(acc, dw[2], dw[3], dppu<0x152>(hq)); mac8(acc, dw[4], dw[5], dppu<0x154>(hq)); mac8(acc, dw[6], dw[7], dppu<0x156>(hq));
        mac8(acc, dw[8], dw[9], dppu<0x158>(hq)); mac8(acc, dw[10], dw[11], dppu<0x15A>(hq)); mac8(acc, dw[12], dw[13], dppu<0x15C>(hq)); mac8(acc, dw[14], dw[15], dppu<0x15E>(hq));
        float a[4];
        UNR for (int e = 0; e < 4; ++e) { a[e] = tr32(acc[e], acc[4 + e]); a[e] = tr16(a[e], a[e]); }
        if (!(lane & 16)) st4(ws, ((lane & 15) >> 2) * DP_S + (lane & 3) * 32 + half * 16, WS_DP + 4 * wave * DP_S + j * 128 + drop, v4u{canon(a[0]), canon(a[1]), canon(a[2]), canon(a[3])});
      }
      STAMP(14);
      if (j < 20 && wave == 0) {
        v4u g[3];
        TPOLL(0, UNR for (int i = 0; i < 3; ++i) g[i] = ld4<AUX_SC1>(ws, lane * 16 + i * 1024, WS_DP + j * DP_S + drop),
              bad = umax4(umax4(umax4(0u, g[0]), g[1]), g[2]) == SENT);
        float a[4];
        UNR for (int e = 0; e < 4; ++e) a[e] = (asf(g[0][e]) + asf(g[1][e])) + asf(g[2][e]);
        float r = tr16(tr32(a[0], a[2]), tr32(a[1], a[3])); r += dpp<ROR8>(r);
        const int c = lane & 31;
        const float dn = rb(asf(__builtin_amdgcn_ds_bpermute(((c >> 2) + (c & 3) * 16) * 4, asu(r))));
        const float o = x2 + rb(gbv * dn);
        if (lane < 32) __builtin_amdgcn_raw_buffer_store_b16((u16)pack2(o, o), rout, lane * 2, 64 * j + drop, 0);
        if (j == 0 && lane == 0) st1(ws, 0, WS_EPOCH + drop, T);
        STAMP(15);
      }
    }
  }
  STAMP_DUMP();
}

extern "C" void kernel_launch(void* const* d_in, const int* in_sizes, int n_in, void* d_out, int out_size, void* d_ws, size_t ws_size, hipStream_t stream) {
  Params p;
  p.x = (const u16*)d_in[0]; p.K = (const u16*)d_in[1]; p.V = (const u16*)d_in[2]; p.wq = (const u16*)d_in[3]; p.wo = (const u16*)d_in[4];
  p.wg = (const u16*)d_in[6]; p.wu = (const u16*)d_in[7]; p.wd = (const u16*)d_in[8]; p.c = (const u16*)d_in[9]; p.wmod = (const u16*)d_in[10];
  p.out = (u16*)d_out; p.ws = (unsigned char*)d_ws;
  for (int i = 0; i < n_in; ++i) { hipPointerAttribute_t a; hipError_t e = hipPointerGetAttributes(&a, d_in[i]); void* base = 0; size_t sz = 0; (void)hipMemGetAddressRange(&base, &sz, d_in[i]); fprintf(stderr, "in[%d] %p size %d type %d managed %d flags %x (err %d) base %p range %zu\n", i, d_in[i], in_sizes[i], (int)a.type, a.isManaged, a.allocationFlags, (int)e, base, sz); }
  { void* base = 0; size_t sz = 0; (void)hipMemGetAddressRange(&base, &sz, d_ws); hipPointerAttribute_t a; (void)hipPointerGetAttributes(&a, d_ws); fprintf(stderr, "out %p ws %p (base %p range %zu flags %x)\n", d_out, d_ws, base, sz, a.allocationFlags); }
  hipLaunchKernelGGL(fused_block, dim3(NWG), dim3(NTHR), 0, stream, p);
}
```

```cpp
#include <hip/hip_runtime.h>
#include <stdint.h>
#pragma clang fp contract(off)

typedef unsigned short u16;
typedef unsigned u32;
typedef unsigned long long u64;
typedef u32 v4u __attribute__((ext_vector_type(4)));
typedef u32 v2u __attribute__((ext_vector_type(2)));
typedef __bf16 bf16x2 __attribute__((ext_vector_type(2)));
#define LAS __attribute__((address_space(3)))
#define GAS __attribute__((address_space(1)))
typedef __amdgpu_buffer_rsrc_t rsrc_t;

constexpr int D = 640, H = 32, SK = 4096, DH = 192, F = 1536, QD = H * DH;
constexpr int NWG = 256, NTHR = 512;
constexpr int AUX_NT = 2, AUX_SC1 = 16;
#define AUX_KV 2
#define AUX_W 2
#define WK 8
#define WV 8
#define AUX_GU 0

constexpr int WS_EPOCH = 0;
constexpr int WS_HG = 4096;
constexpr int WS_MP = WS_HG + 4096;
constexpr int MP_S = 1280 * 8 + 640, OP_S = 768 + 128, BP_S = 4096 + 128, DP_S = 3072 + 128;
constexpr int WS_MG = WS_MP + 8 * MP_S;
constexpr int WS_QP = WS_MG + 2560 * 8;
constexpr int WS_ML = WS_QP + 32 * 8 * 1024;
constexpr int WS_CL = WS_ML + 32 * 32 * 16;
constexpr int WS_SV = (WS_CL + 20 * 128 + 1023) & ~1023;
constexpr int WS_OP = WS_SV;
constexpr int WS_BP = WS_OP + 32 * 7 * OP_S;
constexpr int WS_DP = WS_BP + 10 * BP_S;
constexpr int CL_KB = (WS_DP + 20 * DP_S - WS_SV + 20 * 1024 - 1) / (20 * 1024);
constexpr int WS_END = WS_SV + 20 * CL_KB * 1024;
constexpr int WS_STAMP = 8 << 20;
constexpr int RED0 = 224;
constexpr u32 SENT = 0xFFFFFFFFu;
constexpr int OOB = 0x40000000;
constexpr u32 SPIN_CAP = 1u << 20;

struct Params {
  const u16 *x, *K, *V, *wq, *wo, *wg, *wu, *wd, *c, *wmod;
  u16* out;
  unsigned char* ws;
};

__device__ __forceinline__ float asf(u32 u) { return __builtin_bit_cast(float, u); }
__device__ __forceinline__ u32 asu(float f) { return __builtin_bit_cast(u32, f); }
__device__ __forceinline__ float lo_f(u32 w) { return asf(w << 16); }
__device__ __forceinline__ float hi_f(u32 w) { return asf(w & 0xffff0000u); }
__device__ __forceinline__ u32 pack2(float lo, float hi) { bf16x2 v = {(__bf16)lo, (__bf16)hi}; return __builtin_bit_cast(u32, v); }
__device__ __forceinline__ float rb(float f) { return (float)(__bf16)f; }
__device__ __forceinline__ float dot2(u32 a, u32 b, float c) { return __builtin_amdgcn_fdot2_f32_bf16(__builtin_bit_cast(bf16x2, a), __builtin_bit_cast(bf16x2, b), c, false); }
__device__ __forceinline__ u32 perm_lo(u32 a, u32 b) { return __builtin_amdgcn_perm(b, a, 0x05040100u); }
__device__ __forceinline__ u32 perm_hi(u32 a, u32 b) { return __builtin_amdgcn_perm(b, a, 0x07060302u); }
template <int CTRL> __device__ __forceinline__ float dpp(float x) { return asf(__builtin_amdgcn_mov_dpp(asu(x), CTRL, 0xf, 0xf, true)); }
constexpr int XOR1 = 0xB1, XOR2 = 0x4E, HMIR = 0x141, ROR8 = 0x128;
__device__ __forceinline__ float tr32(float a, float b) { auto s = __builtin_amdgcn_permlane32_swap(asu(a), asu(b), false, false); return asf(s[0]) + asf(s[1]); }
__device__ __forceinline__ float tr16(float a, float b) { auto s = __builtin_amdgcn_permlane16_swap(asu(a), asu(b), false, false); return asf(s[0]) + asf(s[1]); }
__device__ __forceinline__ float tr8(float a, float b, bool bit3) { const float keep = bit3 ? b : a, send = bit3 ? a : b; return keep + dpp<ROR8>(send); }
__device__ __forceinline__ float max32(float a) { auto s = __builtin_amdgcn_permlane32_swap(asu(a), asu(a), false, false); return fmaxf(asf(s[0]), asf(s[1])); }
__device__ __forceinline__ float max16(float a) { auto s = __builtin_amdgcn_permlane16_swap(asu(a), asu(a), false, false); return fmaxf(asf(s[0]), asf(s[1])); }
__device__ __forceinline__ float sum8(float a) { a += dpp<XOR1>(a); a += dpp<XOR2>(a); a += dpp<HMIR>(a); return a; }
__device__ __forceinline__ float sum_hi(float a) { a += dpp<ROR8>(a); a = tr16(a, a); return tr32(a, a); }
__device__ __forceinline__ float wave_sum(float a) { return sum_hi(sum8(a)); }
__device__ __forceinline__ float max_hi(float a) { a = fmaxf(a, dpp<ROR8>(a)); return max32(max16(a)); }
__device__ __forceinline__ float wave_max(float a) { a = fmaxf(a, dpp<XOR1>(a)); a = fmaxf(a, dpp<XOR2>(a)); a = fmaxf(a, dpp<HMIR>(a)); return max_hi(a); }
__device__ __forceinline__ float tr_reduce8(const float* v, int lane) {
  const float a0 = tr32(v[0], v[4]), a1 = tr32(v[1], v[5]), a2 = tr32(v[2], v[6]), a3 = tr32(v[3], v[7]);
  const float b0 = tr16(a0, a2), b1 = tr16(a1, a3);
  return tr8(b0, b1, (lane & 8) != 0);
}
__device__ __forceinline__ rsrc_t mk(const void* p, int bytes) { return __builtin_amdgcn_make_buffer_rsrc((void*)p, 0, bytes, 0x00020000); }
template <int AUX> __device__ __forceinline__ v4u ld4(rsrc_t r, int vo, int so) { return __builtin_amdgcn_raw_buffer_load_b128(r, vo, so, AUX); }
template <int AUX> __device__ __forceinline__ v2u ld2(rsrc_t r, int vo, int so) { return __builtin_amdgcn_raw_buffer_load_b64(r, vo, so, AUX); }
template <int AUX> __device__ __forceinline__ u32 ld1(rsrc_t r, int vo, int so) { return __builtin_amdgcn_raw_buffer_load_b32(r, vo, so, AUX); }
__device__ __forceinline__ void st4(rsrc_t r, int vo, int so, v4u v) { __builtin_amdgcn_raw_buffer_store_b128(v, r, vo, so, AUX_SC1); }
__device__ __forceinline__ void st2(rsrc_t r, int vo, int so, v2u v) { __builtin_amdgcn_raw_buffer_store_b64(v, r, vo, so, AUX_SC1); }
__device__ __forceinline__ void st1(rsrc_t r, int vo, int so, u32 v) { __builtin_amdgcn_raw_buffer_store_b32(v, r, vo, so, AUX_SC1); }
__device__ __forceinline__ u32 umax(u32 a, u32 b) { return a > b ? a : b; }
__device__ __forceinline__ u32 canon(float f) { const u32 u = asu(f); return u < 0xFFFFFFFEu ? u : 0xFFFFFFFEu; }
template <int CTRL> __device__ __forceinline__ u32 dppu(u32 x) { return __builtin_amdgcn_mov_dpp(x, CTRL, 0xf, 0xf, true); }
#define CFENCE() asm volatile("" ::: "memory")
#define LDS_WAIT() asm volatile("s_waitcnt lgkmcnt(0)" ::: "memory")
#define BARRIER() asm volatile("s_waitcnt lgkmcnt(0)\n\ts_barrier" ::: "memory")
#define SCHED() __builtin_amdgcn_sched_barrier(0)
#define KEEPV(x) asm volatile("" : "+v"(x))
__device__ __forceinline__ float frcp(float x) { return __builtin_amdgcn_rcpf(x); }
__device__ __forceinline__ float fsqrt(float x) { return __builtin_amdgcn_sqrtf(x); }
__device__ __forceinline__ float silu(float x) { return x / (1.f + expf(-x)); }
template <int N> __device__ __forceinline__ void vm_wait() { asm volatile("s_waitcnt vmcnt(%0)" :: "i"(N) : "memory"); }
__device__ __forceinline__ bool all_zero(u32 bad) { return __builtin_amdgcn_ballot_w64(bad != 0u) == 0ull; }
__device__ __forceinline__ bool lds_wait_flag(volatile LAS u32* f) {
#pragma unroll 1
  for (u32 sp = 0; sp < (SPIN_CAP << 3); ++sp) {
    if (__builtin_amdgcn_readfirstlane(*f) != 0u) { CFENCE(); return true; }
    __builtin_amdgcn_s_sleep(1);
  }
  return false;
}
#define STAMP(i) do {} while (0)
#define STAMP_DUMP() do {} while (0)
#define STAMP_DECL()

constexpr int L_KF = 0, L_GF = 4, L_PF = 8, L_QF = 12, L_T = 13, L_TF = 14, L_CNT = 15, L_WF = 16, L_HS = 24  , L_QS = 64  ,
              L_PS = 160  , L_SI = 544  , L_RED = 864  , L_GU = 1888  , L_OR = L_RED  , L_X2 = L_SI  , L_ST = L_PS  , L_H2 = L_RED  ,
              L_SPILL = 3168  , L_WORDS = L_SPILL + 8 * 15 * 256;
constexpr float LOG2E = 1.4426950408889634f;
constexpr int NG = 19, NL = 3 * NG;
#define GSTEP 30
#define WOSTEP 56
#define FREE 48
#define XBAR 1
#define WLO 3
#define KT0 60
#define KD 166
#define VD 133
#define PACE(HI, LO, sched) do { if ((u32)__builtin_amdgcn_s_memrealtime() - t_start < (u32)(sched)) vm_wait<LO>(); else vm_wait<HI>(); } while (0)
#define ADELAY 28
#define ASLEEP 2
#define BDELAY 34
#define BSLEEP 2
#define DDELAY 20
#define DSLEEP 1
#define KHI 6
#define KLO 1
#define VHI 6
#define VLO 1
#define K0 1
#define CODE_KB 32
#define BRETRY 0
#define CPF 1
#define HSLEEP 2
#define WK0 3
#define WQW 16
#define WS_ 4
#define DRY 0

#define POLL(SLEEP, LOADS, CHECK) do { u32 sp_ = 0; for (;; ++sp_) { CFENCE(); u32 bad = 0; LOADS; CHECK; if (all_zero(bad)) break; if (sp_ > SPIN_CAP) return; \
    if (SLEEP) __builtin_amdgcn_s_sleep(SLEEP); } } while (0)
#define TPOLL(SLEEP, LOADS, CHECK) do { u32 sp_ = 0; for (;; ++sp_) { CFENCE(); u32 bad = 0; LOADS; CHECK; if (all_zero(bad) || !real) break; if (sp_ > SPIN_CAP) return; \
    if (SLEEP) __builtin_amdgcn_s_sleep(SLEEP); } } while (0)
#define TAG2(v) do { bad |= (v).y ^ T; KEEPV(bad); } while (0)
#define TAG4(v) do { bad |= ((v).y ^ T) | ((v).w ^ T); KEEPV(bad); } while (0)
#define XOR4(v) ((v).x ^ (v).y ^ (v).z ^ (v).w ^ T)
#define UNR _Pragma("unroll")
#define SUM8(a0, a1, a2, a3, a4, a5, a6, a7) ((((a0) + (a1)) + ((a2) + (a3))) + (((a4) + (a5)) + ((a6) + (a7))))
#define ZERO8 {0.f, 0.f, 0.f, 0.f, 0.f, 0.f, 0.f, 0.f}

__device__ __forceinline__ u32 ada_pair(u32 xp, float mb, float rden, float c0, float c1, float s0, float s1) {
  return pack2(rb(rb(rb(lo_f(xp) - mb) * rden) * c0) + s0, rb(rb(rb(hi_f(xp) - mb) * rden) * c1) + s1);
}
__device__ __forceinline__ void ln_stats(const u32* xw, float& mb, float& rden) {
  const float sm = (((lo_f(xw[0]) + hi_f(xw[0])) + (lo_f(xw[1]) + hi_f(xw[1]))) + ((lo_f(xw[2]) + hi_f(xw[2])) + (lo_f(xw[3]) + hi_f(xw[3])))) + (lo_f(xw[4]) + hi_f(xw[4]));
  const float mean = wave_sum(sm) * (1.f / D);
  float sq0 = 0.f, sq1 = 0.f;
  UNR for (int i = 0; i < 5; ++i) { const float d0 = lo_f(xw[i]) - mean, d1 = hi_f(xw[i]) - mean; sq0 = fmaf(d0, d0, sq0); sq1 = fmaf(d1, d1, sq1); }
  const float var = rb(wave_sum(sq0 + sq1) * (1.f / D));
  rden = frcp(rb(fsqrt(rb(var + 1e-5f))));
  mb = rb(mean);
}
__device__ __forceinline__ void pair_rows(v4u& a, v4u& c) { UNR for (int e = 0; e < 4; ++e) { const u32 x = a[e], y = c[e]; a[e] = perm_lo(x, y); c[e] = perm_hi(x, y); } KEEPV(a); KEEPV(c); }
__device__ __forceinline__ void mac8(float* acc, const v4u& lo, const v4u& hi, u32 ap) { UNR for (int e = 0; e < 4; ++e) { acc[2 * e] = dot2(lo[e], ap, acc[2 * e]); acc[2 * e + 1] = dot2(hi[e], ap, acc[2 * e + 1]); } }
typedef float f32x4 __attribute__((ext_vector_type(4)));
typedef __bf16 bf16x8 __attribute__((ext_vector_type(8)));
__device__ __forceinline__ f32x4 mfma(const v4u& a, const v4u& b, f32x4 c) { return __builtin_amdgcn_mfma_f32_16x16x32_bf16(__builtin_bit_cast(bf16x8, a), __builtin_bit_cast(bf16x8, b), c, 0, 0, 0); }
__device__ __forceinline__ void to_frags(v4u* r) {
  v4u f[8];
  UNR for (int e = 0; e < 4; ++e) UNR for (int k = 0; k < 4; ++k) { f[2 * e][k] = perm_lo(r[2 * k][e], r[2 * k + 1][e]); f[2 * e + 1][k] = perm_hi(r[2 * k][e], r[2 * k + 1][e]); }
  UNR for (int e = 0; e < 8; ++e) { r[e] = f[e]; KEEPV(r[e]); }
}
__device__ __forceinline__ float sum16(float a) { a = sum8(a); return a + dpp<ROR8>(a); }
__device__ __forceinline__ u32 umax4(u32 m, const v4u& g) { return umax(umax(umax(m, g.x), g.y), umax(g.z, g.w)); }

__global__ void __launch_bounds__(NTHR, 2) fused_block(Params p) {
  __shared__ __attribute__((aligned(16))) u32 smem[L_WORDS];
  LAS u32* L = (LAS u32*)smem;
  volatile LAS u32* LV = (volatile LAS u32*)smem;
  const int tid = threadIdx.x, lane = tid & 63, wave = __builtin_amdgcn_readfirstlane(tid >> 6), b = blockIdx.x;
  const int h = (b & 7) * 4 + ((b >> 3) & 3), s = b >> 5, r8 = lane >> 3, c8 = lane & 7, w = wave & 3;
  const bool svc = s == 7, mlp = svc && (h & 3) != 3;
  const int j = 3 * (h >> 2) + (h & 3), xi = h >> 2;
  const rsrc_t ws = mk(p.ws, WS_END);
  const int l40 = lane < 40 ? lane : 39;
  u32 T = 0;
  const u32 t_start = (u32)__builtin_amdgcn_s_memrealtime();
  u64 pc0;
  asm volatile("s_getpc_b64 %0" : "=s"(pc0));
  u32 cpf = 0;
  if (CPF) {
    cpf = ld1<0>(mk((const void*)(pc0 & ~127ull), CODE_KB * 1024), lane < 2 ? lane * 32768 : OOB, ((b >> 3) * 8 + wave) * 128);
    SCHED();
  }
  STAMP_DECL();
  STAMP(0);
  const rsrc_t rm = mk(p.wmod, D * 6 * D * 2), rc = mk(p.c, D * 2);
#define MKTAG(e) (__builtin_amdgcn_readfirstlane(e) * 0x9E3779B1u + 0x7F4A7C15u)
#define MOD_LD(i, rowblk, colbyte) ld4<AUX_W>(rm, r8 * 2 * 7680 + c8 * 16, (80 * (rowblk) + 16 * ((i) >> 1) + ((i) & 1)) * 7680 + (colbyte))
#define MOD_SILU(cw, sp) do { L[L_SI + wave * 40 + l40] = pack2(silu(lo_f(cw)), silu(hi_f(cw))); LDS_WAIT(); UNR for (int i = 0; i < 5; ++i) sp[i] = L[L_SI + wave * 40 + 8 * i + r8]; } while (0)
#define MOD_MAC(res, m, sp) do { float acc_[8] = ZERO8; \
    UNR for (int i = 0; i < 5; ++i) { pair_rows(m[2 * i], m[2 * i + 1]); mac8(acc_, m[2 * i], m[2 * i + 1], sp[i]); } res = tr_reduce8(acc_, lane); } while (0)
  if (wave == 5 && b < 160) {
    __builtin_amdgcn_s_setprio(3);
    const int blk = b >> 3, rblk = b & 7;
    u32 sp[5]; v4u m0[10];
    const u32 cw = ld1<0>(rc, l40 * 4, rblk * 160);
    UNR for (int i = 0; i < 10; ++i) m0[i] = MOD_LD(i, rblk, 128 * blk);
    const u32 E = ld1<AUX_SC1>(ws, 0, WS_EPOCH);
    SCHED();
    BARRIER();
    MOD_SILU(cw, sp);
    float v;
    MOD_MAC(v, m0, sp);
    const u32 Tq = MKTAG(E), vn = dppu<ROR8>(asu(v));
    if (!(lane & 8)) st4(ws, (8 * c8 + r8) * 8, WS_MP + rblk * MP_S + 512 * blk, v4u{asu(v), Tq, vn, Tq});
    __builtin_amdgcn_s_setprio(0);
    STAMP(1);
  } else if (wave == 5 && b < 180) {
    BARRIER();
    const int jc = b - 160;
    const u32 E = ld1<AUX_SC1>(ws, 0, WS_EPOCH);
    const v4u sv = {SENT, SENT, SENT, SENT};
    UNR for (int i = 0; i < CL_KB; ++i) { if (i >= 8) vm_wait<7>(); st4(ws, lane * 16, WS_SV + (jc * CL_KB + i) * 1024, sv); SCHED(); }
    const u32 Tq = MKTAG(E);
    vm_wait<0>();
    if (lane == 0) st4(ws, 0, WS_CL + jc * 128, v4u{0u, Tq, 0u, Tq});
    STAMP(2);
  } else if (!svc && (wave == 4 || (wave == 6 && s == 6))) {
    __builtin_amdgcn_s_setprio(3);
    const int sl = wave == 4 ? s : 7;
    LAS u32* hs = L + (wave == 4 ? L_HS : L_RED);
    BARRIER();
    const rsrc_t rqw = mk(p.wq, D * QD * 2);
    v4u qw[30];
    UNR for (int i = 0; i < 30; ++i) {
      if (i >= 15 && i % 3 == 0) vm_wait<12>();
      qw[10 * (i % 3) + i / 3] = ld4<AUX_W>(rqw, r8 * 2 * 12288 + c8 * 16 + (i % 3) * 128, (80 * sl + 16 * ((i / 3) >> 1) + ((i / 3) & 1)) * 12288 + h * DH * 2);
      SCHED();
    }
    const u32 E = ld1<AUX_SC1>(ws, 0, WS_EPOCH);
    SCHED();
    UNR for (int i = 0; i < 15; ++i) pair_rows(qw[2 * i], qw[2 * i + 1]);
    T = MKTAG(E);
    if (wave == 4) { L[L_T] = T; LDS_WAIT(); LV[L_TF] = 1u; }
    STAMP(3);
    {
      v2u g;
      POLL(HSLEEP, g = ld2<AUX_SC1>(ws, l40 * 8, WS_HG + sl * 320), TAG2(g));
      hs[l40] = g.x;
      LDS_WAIT();
    }
    STAMP(4);
    {
      u32 hp[5];
      UNR for (int i = 0; i < 5; ++i) hp[i] = hs[8 * i + r8];
      u32 qv[3];
      UNR for (int t = 0; t < 3; ++t) {
        float acc[8] = ZERO8;
        UNR for (int i = 0; i < 5; ++i) mac8(acc, qw[10 * t + 2 * i], qw[10 * t + 2 * i + 1], hp[i]);
        qv[t] = asu(tr_reduce8(acc, lane));
      }
      st4(ws, lane * 16, WS_QP + (h * 8 + sl) * 1024, v4u{qv[0], qv[1], qv[2], qv[0] ^ qv[1] ^ qv[2] ^ T});
    }
    __builtin_amdgcn_s_setprio(0);
    STAMP(5);
  } else if (svc && !mlp && wave == 0) {
    const rsrc_t rx = mk(p.x, D * 2);
    u32 xw[5];
    UNR for (int i = 0; i < 5; ++i) xw[i] = ld1<0>(rx, lane * 4 + i * 256, 0);
    const u32 xme = ld1<0>(rx, l40 * 4, xi * 160);
    const u32 E = ld1<AUX_SC1>(ws, 0, WS_EPOCH);
    SCHED();
    BARRIER();
    float mb, rden;
    ln_stats(xw, mb, rden);
    T = MKTAG(E);
    v4u g[16];
    POLL(0, UNR for (int q = 0; q < 16; ++q) g[q] = ld4<AUX_SC1>(ws, l40 * 16, WS_MP + (q & 7) * MP_S + ((q >> 3) * 640 + 80 * xi) * 8),
         UNR for (int q = 0; q < 16; ++q) TAG4(g[q]));
    float v[4];
    UNR for (int k = 0; k < 4; ++k) { const int o = 8 * (k >> 1), e = 2 * (k & 1);
      v[k] = rb(SUM8(asf(g[o][e]), asf(g[o + 1][e]), asf(g[o + 2][e]), asf(g[o + 3][e]), asf(g[o + 4][e]), asf(g[o + 5][e]), asf(g[o + 6][e]), asf(g[o + 7][e]))); }
    const u32 hp = ada_pair(xme, mb, rden, rb(1.f + v[2]), rb(1.f + v[3]), v[0], v[1]), hn = dppu<XOR1>(hp);
    if (lane < 40 && !(lane & 1)) st4(ws, lane * 8, WS_HG + xi * 320, v4u{hp, T, hn, T});
    LV[L_QF] = 1u;
    STAMP(1);
  } else {
    if (wave == 7 && lane < 24) L[lane] = 0u;
    BARRIER();
  }
  const int wid = (3 - w) * 7 + s;
  const int kvoff = r8 * 384 + c8 * 16, kvbase = (h * 512 + wid) * 3072;
  const int lastbase = wid < 8 ? kvbase + 18 * 28 * 3072 : OOB;
  LAS v4u* spill = (LAS v4u*)(L + L_SPILL + wave * 15 * 256) + lane;
  if (!svc && wave >= 4) {
    if (!lds_wait_flag(LV + L_KF + w)) return;
    asm volatile("" :: "v"(cpf));
    STAMP(7);
    SCHED();
    const rsrc_t rv = mk(p.V, H * SK * DH * 2);
    v4u vr[42];
#define VR(i) vr[(i) % 42]
#define VSLOT(i) ((i) < 3 ? (i) : (i) >= 51 ? (i) : 3 + 6 * (((i) - 3) / 6) + 2 * (((i) - 3) % 3) + ((((i) - 3) / 3) & 1))
#define VISSUE(i) do { if ((i) % 3 == 0 && (i) >= 3 && (i) < FREE) PACE(VHI, VLO, KT0 + (57 * KD + (i) * VD) / 10); \
      VR(VSLOT(i)) = (i) < 3 ? ld4<AUX_KV>(rv, kvoff + (i) * 128, lastbase) \
                      : ld4<AUX_KV>(rv, kvoff + (((VSLOT(i) + 3) % 6) >> 1) * 128, kvbase + (2 * ((VSLOT(i) - 3) / 6) + ((VSLOT(i) + 1) & 1)) * (28 * 3072)); SCHED(); \
      if ((i) < FREE && (i) >= 17 && ((i) - 17) % 6 == 0) { UNR for (int k = 0; k < 3; ++k) pair_rows(VR((i) - 14 + 2 * k), VR((i) - 13 + 2 * k)); } \
      if ((i) == 23) { UNR for (int k = 0; k < 15; ++k) spill[64 * k] = VR(k); } \
      if ((i) == GSTEP) LV[L_GF + w] = 1u; \
      if ((i) == WOSTEP) LV[L_WF + w] = 1u; SCHED(); } while (0)
    static_assert(VHI <= 6 && KHI <= 9 && FREE == 48, "pairing / spill points assume these");
    UNR for (int i = 0; i < 30; ++i) VISSUE(i);
    UNR for (int i = 30; i < NL; ++i) VISSUE(i);
    STAMP(8);
    if (!lds_wait_flag(LV + L_PF + w)) return;
    STAMP(9);
    u32 pp[12];
    UNR for (int k = 0; k < 3; ++k) { const v4u t4 = *(const LAS v4u*)(L + L_PS + (w * 8 + r8) * 12 + 4 * k); UNR for (int e = 0; e < 4; ++e) pp[4 * k + e] = t4[e]; }
    float acc[24];
    UNR for (int i = 0; i < 24; ++i) acc[i] = 0.f;
    constexpr int LASTP = 38;
#define PV_T(i, t) do { if (4 + 6 * (i) + 2 * (t) > LASTP) pair_rows(VR(3 + 6 * (i) + 2 * (t)), VR(4 + 6 * (i) + 2 * (t))); \
      mac8(acc + 8 * (t), VR(3 + 6 * (i) + 2 * (t)), VR(4 + 6 * (i) + 2 * (t)), pp[i]); } while (0)
#define PV(i) do { PV_T(i, 0); PV_T(i, 1); PV_T(i, 2); SCHED(); } while (0)
    PV(2); PV(3);
    {
      v4u sv[9];
      UNR for (int k = 0; k < 9; ++k) sv[k] = spill[64 * k];
      SCHED();
      PV(4);
      UNR for (int t = 0; t < 3; ++t) {
        UNR for (int e = 0; e < 4; ++e) { acc[8 * t + 2 * e] = dot2(sv[t][e] & 0xffffu, pp[9], acc[8 * t + 2 * e]); acc[8 * t + 2 * e + 1] = dot2(sv[t][e] & 0xffff0000u, pp[10], acc[8 * t + 2 * e + 1]); }
        mac8(acc + 8 * t, sv[3 + 2 * t], sv[4 + 2 * t], pp[0]);
      }
      SCHED();
      UNR for (int k = 0; k < 6; ++k) sv[k] = spill[64 * (9 + k)];
      SCHED();
      PV(5);
      UNR for (int t = 0; t < 3; ++t) mac8(acc + 8 * t, sv[2 * t], sv[2 * t + 1], pp[1]);
      SCHED();
    }
    PV(6);
    STAMP(10);
    PV(7);
    PV_T(8, 0);
    L[L_OR + w * 192 + 8 * c8 + r8] = asu(tr_reduce8(acc, lane));
    SCHED();
    PV_T(8, 1);
    L[L_OR + w * 192 + 64 + 8 * c8 + r8] = asu(tr_reduce8(acc + 8, lane));
    SCHED();
    PV_T(8, 2);
    const float o2 = tr_reduce8(acc + 16, lane);
    L[L_OR + w * 192 + 128 + 8 * c8 + r8] = asu(o2);
    STAMP(11);
    BARRIER();
    if (w == 0 && lane < 48) {
      const v4u t0 = *(const LAS v4u*)(L + L_OR + 4 * lane), t1 = *(const LAS v4u*)(L + L_OR + 192 + 4 * lane), t2 = *(const LAS v4u*)(L + L_OR + 384 + 4 * lane), t3 = *(const LAS v4u*)(L + L_OR + 576 + 4 * lane);
      v4u r;
      UNR for (int e = 0; e < 4; ++e) r[e] = canon((asf(t0[e]) + asf(t1[e])) + (asf(t2[e]) + asf(t3[e])));
      st4(ws, lane * 16, WS_OP + (h * 7 + s) * OP_S, r);
    }
    STAMP(12);
    STAMP_DUMP();
    return;
  }
  if (!svc) {
    const rsrc_t rk = mk(p.K, H * SK * DH * 2);
    v4u kr[45];
#define KR(i) kr[(i) % 45]
#define KISSUE(i) do { if ((i) % 3 == 0) { if ((i) >= 9) PACE(KHI, KLO, KT0 + (i) * KD / 10); else if ((i) >= 3) vm_wait<K0>(); } \
      KR(i) = ld4<AUX_KV>(rk, kvoff + ((i) % 3) * 128, (i) < 54 ? kvbase + ((i) / 3) * (28 * 3072) : lastbase); SCHED(); \
      if ((i) == 23) { UNR for (int k = 0; k < 12; ++k) spill[64 * k] = KR(k); SCHED(); } } while (0)
    UNR for (int i = 0; i < NL; ++i) KISSUE(i);
    asm volatile("" :: "v"(cpf));
    LV[L_KF + w] = 1u;
    STAMP(1);
    if (!lds_wait_flag(LV + L_TF)) return;
    T = __builtin_amdgcn_readfirstlane(L[L_T]);
    if (w == 0) {
      v4u g[8];
      POLL(4, UNR for (int q = 0; q < 8; ++q) g[q] = ld4<AUX_SC1>(ws, lane * 16, WS_QP + (h * 8 + q) * 1024),
           UNR for (int i = 0; i < 8; ++i) { bad |= XOR4(g[i]); KEEPV(bad); });
      LAS u16* qs = (LAS u16*)(L + L_QS);
      UNR for (int t = 0; t < 3; ++t) {
        const float q = SUM8(asf(g[0][t]), asf(g[1][t]), asf(g[2][t]), asf(g[3][t]), asf(g[4][t]), asf(g[5][t]), asf(g[6][t]), asf(g[7][t]));
        qs[64 * t + 8 * c8 + r8] = (u16)pack2(q, q);
      }
      LDS_WAIT();
      LV[L_QF] = 1u;
    } else if (!lds_wait_flag(LV + L_QF)) return;
    CFENCE();
    STAMP(2);
    v4u qp[3];
    float sc[NG];
    UNR for (int t = 0; t < 3; ++t) qp[t] = *(const LAS v4u*)(L + L_QS + 32 * t + 4 * c8);
#define QK(g, k0, k1, k2) do { float a_ = 0.f; UNR for (int e = 0; e < 4; ++e) a_ = dot2((k0)[e], qp[0][e], a_); UNR for (int e = 0; e < 4; ++e) a_ = dot2((k1)[e], qp[1][e], a_); \
      UNR for (int e = 0; e < 4; ++e) a_ = dot2((k2)[e], qp[2][e], a_); sc[g] = rb(rb(sum8(a_)) * 0.07216878364870322f); } while (0)
    UNR for (int g = 4; g < NG; ++g) QK(g, KR(3 * g), KR(3 * g + 1), KR(3 * g + 2));
    SCHED();
    UNR for (int g = 0; g < 4; ++g) { const v4u k0 = spill[64 * (3 * g)], k1 = spill[64 * (3 * g + 1)], k2 = spill[64 * (3 * g + 2)]; QK(g, k0, k1, k2); }
    if (wid >= 8) sc[18] = -INFINITY;
    float m = sc[0];
    UNR for (int g = 1; g < NG; ++g) m = fmaxf(m, sc[g]);
    m = max_hi(m);
    float l = 0.f;
    UNR for (int g = 0; g < NG; ++g) l += __builtin_amdgcn_exp2f((sc[g] - m) * LOG2E);
    l = sum_hi(l);
    if (lane == 0) st4(ws, 0, WS_ML + (h * 32 + wid) * 16, v4u{asu(m), T, asu(l), T});
    STAMP(4);
    if (!lds_wait_flag(LV + L_GF + w)) return;
    STAMP(5);
    float M, Ls;
    {
      const int l32 = lane & 31;
      const int vo = lane < 32 ? WS_ML + h * 512 + (l32 < 28 ? l32 : 0) * 16 : WS_CL + (l32 < 20 ? l32 : 0) * 128;
      v4u g;
      POLL(8, g = ld4<AUX_SC1>(ws, vo, 0), TAG4(g));
      const float mm = lane < 28 ? asf(g.x) : -INFINITY;
      M = wave_max(mm);
      Ls = wave_sum(lane < 28 ? asf(g.z) * __builtin_amdgcn_exp2f((mm - M) * LOG2E) : 0.f);
    }
    {
      const float inv = 1.f / Ls;
      v4u pp[3];
      UNR for (int i = 0; i < 9; ++i)
        pp[i >> 2][i & 3] = pack2(__builtin_amdgcn_exp2f((sc[2 * i] - M) * LOG2E) * inv, __builtin_amdgcn_exp2f((sc[2 * i + 1] - M) * LOG2E) * inv);
      const float pl = __builtin_amdgcn_exp2f((sc[18] - M) * LOG2E) * inv;
      pp[2][1] = pack2(pl, 0.f); pp[2][2] = pack2(0.f, pl); pp[2][3] = 0u;
      if (c8 == 0) { UNR for (int k = 0; k < 3; ++k) *(LAS v4u*)(L + L_PS + (w * 8 + r8) * 12 + 4 * k) = pp[k]; }
      LDS_WAIT();
      LV[L_PF + w] = 1u;
    }
    STAMP(6);
    const int li = b >> 3;
    if (li >= 20) { BARRIER(); STAMP_DUMP(); return; }
    const int rg = 2 * (b & 7) + (li >= 10), cb = li >= 10 ? li - 10 : li, hh = 2 * rg + (w >> 1), d0 = 96 * (w & 1);
    UNR for (int k = 0; k < 4; ++k) if (!lds_wait_flag(LV + L_WF + k)) return;
    STAMP(7);
    v4u ow[12];
    {
      const rsrc_t ro = mk(p.wo, QD * D * 2);
      UNR for (int i = 0; i < 12; ++i) ow[i] = ld4<AUX_W>(ro, r8 * 12 * 1280 + c8 * 16, (hh * DH + d0 + i) * 1280 + cb * 128);
      SCHED();
    }
    BARRIER();
    STAMP(8);
    if (ADELAY) __builtin_amdgcn_s_sleep(ADELAY);
    {
      v4u g[3];
      const int vo = c8 < 7 ? c8 * OP_S + r8 * 48 : OOB;
      UNR for (int k = 0; k < 3; ++k) g[k] = ld4<AUX_SC1>(ws, vo + 16 * k, WS_OP + hh * 7 * OP_S + d0 * 4);
      SCHED();
      UNR for (int i = 0; i < 6; ++i) pair_rows(ow[2 * i], ow[2 * i + 1]);
      STAMP(9);
      if (!all_zero(umax4(umax4(umax4(0u, g[0]), g[1]), g[2]) == SENT)) {
        POLL(ASLEEP, UNR for (int k = 0; k < 3; ++k) g[k] = ld4<AUX_SC1>(ws, vo + 16 * k, WS_OP + hh * 7 * OP_S + d0 * 4),
             bad = umax4(umax4(umax4(0u, g[0]), g[1]), g[2]) == SENT);
      }
      STAMP(10);
      float acc[8] = ZERO8;
      UNR for (int i = 0; i < 6; ++i) mac8(acc, ow[2 * i], ow[2 * i + 1], pack2(sum8(asf(g[i >> 1][2 * (i & 1)])), sum8(asf(g[i >> 1][2 * (i & 1) + 1]))));
      L[L_GU + w * 64 + 8 * c8 + r8] = asu(tr_reduce8(acc, lane));
      BARRIER();
      if (w == 0 && lane < 16) {
        const v4u t0 = *(const LAS v4u*)(L + L_GU + 4 * lane), t1 = *(const LAS v4u*)(L + L_GU + 64 + 4 * lane), t2 = *(const LAS v4u*)(L + L_GU + 128 + 4 * lane), t3 = *(const LAS v4u*)(L + L_GU + 192 + 4 * lane);
        v4u r;
        UNR for (int e = 0; e < 4; ++e) r[e] = canon((asf(t0[e]) + asf(t1[e])) + (asf(t2[e]) + asf(t3[e])));
        st4(ws, lane * 16, WS_BP + cb * BP_S + rg * 256, r);
      }
    }
    STAMP(11);
    STAMP_DUMP();
    return;
  }
  {
    if (!mlp && !lds_wait_flag(LV + L_QF)) return;
    {
      u32 sp[5]; v4u m0[10], m1[10];
      const u32 cw = ld1<0>(rc, l40 * 4, wave * 160);
      const u32 E = ld1<AUX_SC1>(ws, 0, WS_EPOCH);
      UNR for (int i = 0; i < 10; ++i) m0[i] = MOD_LD(i, wave, 2560 + 128 * h);
      SCHED();
      if (!mlp) { UNR for (int i = 0; i < 10; ++i) m1[i] = MOD_LD(i, wave, 2560 + 128 * (32 + xi)); }
      SCHED();
      MOD_SILU(cw, sp);
      asm volatile("" :: "v"(cpf));
      T = MKTAG(E);
      float v0;
      MOD_MAC(v0, m0, sp);
      L[L_RED + wave * 64 + 8 * c8 + r8] = asu(v0);
      if (!mlp) { MOD_MAC(v0, m1, sp); L[L_RED + 512 + wave * 64 + 8 * c8 + r8] = asu(v0); }
      BARRIER();
      if (wave == 7 || (wave == 6 && !mlp)) {
        const LAS u32* a = L + L_RED + (7 - wave) * 512 + lane;
        const u32 v = asu(rb(SUM8(asf(a[0]), asf(a[64]), asf(a[128]), asf(a[192]), asf(a[256]), asf(a[320]), asf(a[384]), asf(a[448])))), vn = dppu<XOR1>(v);
        if (!(lane & 1)) st4(ws, lane * 8, WS_MG + (wave == 6 ? 32 + xi : h) * 512, v4u{v, T, vn, T});
      }
    }
    STAMP(2);
    if (!mlp) {
      if (CODE_KB) {
        const rsrc_t rcode = mk((const void*)(pc0 & ~127ull), CODE_KB * 1024);
        u32 acc = 0;
        UNR for (int i = 0; i < (CODE_KB + 7) / 8; ++i) acc |= ld4<0>(rcode, lane * 16, (wave + 8 * i) * 1024).x;
        asm volatile("" :: "v"(acc));
      }
      STAMP_DUMP(); return;
    }
    const int cg = lane & 15, rq = lane >> 4;
    v4u wf[40];
    if (wave < 4) {
      const GAS v4u* gp = (const GAS v4u*)((cg < 8 ? p.wg : p.wu) + (size_t)(160 * w + 8 * rq) * F + 64 * j + 8 * (cg & 7));
      UNR for (int i = 0; i < 40; ++i) wf[i] = __builtin_nontemporal_load(gp + (32 * (i >> 3) + (i & 7)) * (F / 8));
      SCHED();
      UNR for (int c = 0; c < 5; ++c) to_frags(wf + 8 * c);
    } else {
      const rsrc_t rd = mk(p.wd, F * D * 2);
      u32 ex[16];
      UNR for (int i = 0; i < 16; ++i) { wf[i] = ld4<AUX_W>(rd, rq * 16 * 1280 + cg * 16, (64 * j + i) * 1280 + 256 * w); ex[i] = ld1<AUX_W>(rd, rq * 16 * 1280 + cg * 4, (64 * j + i) * 1280 + 1024 + 64 * w); }
      SCHED();
      to_frags(wf); to_frags(wf + 8);
      UNR for (int c = 0; c < 2; ++c) UNR for (int k = 0; k < 4; ++k) { wf[16 + 2 * c][k] = perm_lo(ex[8 * c + 2 * k], ex[8 * c + 2 * k + 1]); wf[17 + 2 * c][k] = perm_hi(ex[8 * c + 2 * k], ex[8 * c + 2 * k + 1]); }
      UNR for (int i = 16; i < 20; ++i) KEEPV(wf[i]);
    }
    STAMP(3);
    const bool two = (wave & 6) == 4;
    const int cc = 4 * cg + rq, col0 = 64 * wave + cc, col1 = 512 + 64 * (wave & 1) + cc, colf = (j < 20 ? 32 * j : 0) + 4 * c8 + rq;
    const float xv0 = lo_f(p.x[col0]), xv1 = lo_f(p.x[col1]);
    float ga0, ga1, sb0, sb1, cb0, cb1, gbv;
    {
      v2u g[8];
      POLL(16, UNR for (int k = 0; k < 3; ++k) { g[2 * k] = ld2<AUX_SC1>(ws, col0 * 8, WS_MG + 640 * 8 * k); g[2 * k + 1] = ld2<AUX_SC1>(ws, col1 * 8, WS_MG + 640 * 8 * k); }
           g[6] = ld2<AUX_SC1>(ws, colf * 8, WS_MG + 1920 * 8); g[7] = ld2<AUX_SC1>(ws, (lane < 20 ? lane : 0) * 128, WS_CL),
           UNR for (int i = 0; i < 8; ++i) TAG2(g[i]));
      ga0 = asf(g[0].x); ga1 = asf(g[1].x); sb0 = asf(g[2].x); sb1 = asf(g[3].x); cb0 = rb(1.f + asf(g[4].x)); cb1 = rb(1.f + asf(g[5].x)); gbv = asf(g[6].x);
    }
    STAMP(6);
    if (wave == 7) {
      u32 g;
      POLL(4, g = ld1<AUX_SC1>(ws, (lane < 7 ? lane : 0) * OP_S + 764, WS_OP + h * 7 * OP_S), bad = g == SENT);
    }
    BARRIER();
    STAMP(9);
    if (BDELAY) __builtin_amdgcn_s_sleep(BDELAY);
    STAMP(10);
    float x20, x21 = 0.f;
    {
      v4u g[8];
      POLL(BSLEEP, UNR for (int i = 0; i < 4; ++i) g[i] = ld4<AUX_SC1>(ws, lane * 16 + i * 1024, WS_BP + wave * BP_S);
           if (two) { UNR for (int i = 0; i < 4; ++i) g[4 + i] = ld4<AUX_SC1>(ws, lane * 16 + i * 1024, WS_BP + (4 + wave) * BP_S); },
           u32 mx = umax(umax4(umax4(0u, g[0]), g[1]), umax4(umax4(0u, g[2]), g[3]));
           if (two) mx = umax(umax4(umax4(mx, g[4]), g[5]), umax4(umax4(0u, g[6]), g[7])); bad = mx == SENT);
      STAMP(11);
      UNR for (int k = 0; k < 2; ++k) {
        if (k == 0 || two) {
          float a[4];
          UNR for (int e = 0; e < 4; ++e) a[e] = (asf(g[4 * k][e]) + asf(g[4 * k + 1][e])) + (asf(g[4 * k + 2][e]) + asf(g[4 * k + 3][e]));
          const float at = rb(tr16(tr32(a[0], a[2]), tr32(a[1], a[3])));
          const float x2 = rb((k ? xv1 : xv0) + rb((k ? ga1 : ga0) * at));
          (k ? x21 : x20) = x2;
          const float pv = asf(__builtin_amdgcn_readfirstlane(asu(x2))), d = x2 - pv;
          const float s1 = wave_sum(d), sq = wave_sum(d * d);
          *(LAS v2u*)(L + L_ST + 2 * (wave + 4 * k)) = v2u{asu(fmaf(s1, 1.f / 64, pv)), asu(fmaf(-s1 * (1.f / 64), s1, sq))};
          ((LAS u16*)(L + L_X2))[k ? col1 : col0] = (u16)(asu(x2) >> 16);
        }
      }
      BARRIER();
    }
    STAMP(12);
    {
      const v2u st = *(const LAS v2u*)(L + L_ST + 2 * (cg < 10 ? cg : 0));
      const float mean = sum16(cg < 10 ? asf(st.x) : 0.f) * 0.1f, e = asf(st.x) - mean;
      const float var = rb(sum16(cg < 10 ? fmaf(e * 64.f, e, asf(st.y)) : 0.f) * (1.f / D));
      const float rden = frcp(rb(fsqrt(rb(var + 1e-5f)))), mb = rb(mean);
      const float h0 = rb(rb(rb(x20 - mb) * rden) * cb0) + sb0;
      ((LAS u16*)(L + L_H2))[col0] = (u16)pack2(h0, h0);
      if (two) { const float h1 = rb(rb(rb(x21 - mb) * rden) * cb1) + sb1; ((LAS u16*)(L + L_H2))[col1] = (u16)pack2(h1, h1); }
      BARRIER();
    }
    STAMP(7);
    if (wave < 4) {
      v4u af[5];
      UNR for (int c = 0; c < 5; ++c) af[c] = *(const LAS v4u*)(L + L_H2 + 80 * w + 16 * c + 4 * rq);
      f32x4 acc[8];
      UNR for (int e = 0; e < 8; ++e) acc[e] = f32x4{0.f, 0.f, 0.f, 0.f};
      UNR for (int c = 0; c < 5; ++c) UNR for (int e = 0; e < 8; ++e) acc[e] = mfma(af[c], wf[8 * c + e], acc[e]);
      if (lane < 16) {
        *(LAS v4u*)(L + L_GU + w * 128 + 8 * cg) = v4u{asu(acc[0][0]), asu(acc[1][0]), asu(acc[2][0]), asu(acc[3][0])};
        *(LAS v4u*)(L + L_GU + w * 128 + 8 * cg + 4) = v4u{asu(acc[4][0]), asu(acc[5][0]), asu(acc[6][0]), asu(acc[7][0])};
      }
    }
    BARRIER();
    STAMP(13);
    if (wave >= 4) {
      const LAS u32* a = L + L_GU + lane;
      const float gg = rb((asf(a[0]) + asf(a[128])) + (asf(a[256]) + asf(a[384]))), uu = rb((asf(a[64]) + asf(a[192])) + (asf(a[320]) + asf(a[448])));
      const float hh = gg * uu;
      const u32 hq = pack2(hh, dpp<XOR1>(hh));
      const v4u a0 = {dppu<0x150>(hq), dppu<0x152>(hq), dppu<0x154>(hq), dppu<0x156>(hq)}, a1 = {dppu<0x158>(hq), dppu<0x15A>(hq), dppu<0x15C>(hq), dppu<0x15E>(hq)};
      f32x4 acc[10];
      UNR for (int e = 0; e < 10; ++e) acc[e] = mfma(a0, wf[e < 8 ? e : 8 + e], f32x4{0.f, 0.f, 0.f, 0.f});
      UNR for (int e = 0; e < 10; ++e) acc[e] = mfma(a1, wf[e < 8 ? 8 + e : 10 + e], acc[e]);
      v4u r;
      UNR for (int e = 0; e < 4; ++e) r[e] = canon(rq == 0 ? acc[e][0] : acc[4 + e][0]);
      if (rq < 2) st4(ws, (cg >> 2) * DP_S + (cg & 3) * 32 + rq * 16, WS_DP + 4 * w * DP_S + j * 128, r);
      if (rq == 2) st2(ws, cg * 8, WS_DP + (16 + w) * DP_S + j * 128, v2u{canon(acc[8][0]), canon(acc[9][0])});
      STAMP(14);
    } else if (j < 20 && wave == 3) {
      const float x2 = lo_f(((const LAS u16*)(L + L_X2))[colf]);
      if (DDELAY) __builtin_amdgcn_s_sleep(DDELAY);
      v4u g[3];
      POLL(DSLEEP, UNR for (int i = 0; i < 3; ++i) g[i] = ld4<AUX_SC1>(ws, lane * 16 + i * 1024, WS_DP + j * DP_S),
           bad = umax4(umax4(umax4(0u, g[0]), g[1]), g[2]) == SENT);
      float a[4];
      UNR for (int e = 0; e < 4; ++e) a[e] = (asf(g[0][e]) + asf(g[1][e])) + asf(g[2][e]);
      float r = tr16(tr32(a[0], a[2]), tr32(a[1], a[3])); r += dpp<ROR8>(r);
      const float o = x2 + rb(gbv * rb(r));
      if (!(lane & 8)) p.out[colf] = (u16)pack2(o, o);
      if (j == 0 && lane == 0) st1(ws, 0, WS_EPOCH, T);
      STAMP(15);
    }
  }
  STAMP_DUMP();
}

extern "C" void kernel_launch(void* const* d_in, const int* in_sizes, int n_in, void* d_out, int out_size, void* d_ws, size_t ws_size, hipStream_t stream) {
  Params p;
  p.x = (const u16*)d_in[0]; p.K = (const u16*)d_in[1]; p.V = (const u16*)d_in[2]; p.wq = (const u16*)d_in[3]; p.wo = (const u16*)d_in[4];
  p.wg = (const u16*)d_in[6]; p.wu = (const u16*)d_in[7]; p.wd = (const u16*)d_in[8]; p.c = (const u16*)d_in[9]; p.wmod = (const u16*)d_in[10];
  p.out = (u16*)d_out; p.ws = (unsigned char*)d_ws;
  hipLaunchKernelGGL(fused_block, dim3(NWG), dim3(NTHR), 0, stream, p);
}
```

```cpp
#include <hip/hip_runtime.h>
#include <stdint.h>
#pragma clang fp contract(off)

typedef unsigned short u16;
typedef unsigned u32;
typedef unsigned long long u64;
typedef u32 v4u __attribute__((ext_vector_type(4)));
typedef u32 v2u __attribute__((ext_vector_type(2)));
typedef __bf16 bf16x2 __attribute__((ext_vector_type(2)));
#define LAS __attribute__((address_space(3)))
#define GAS __attribute__((address_space(1)))
typedef __amdgpu_buffer_rsrc_t rsrc_t;

constexpr int D = 640, H = 32, SK = 4096, DH = 192, F = 1536, QD = H * DH;
constexpr int NWG = 256, NTHR = 512;
constexpr int AUX_NT = 2, AUX_SC1 = 16;
#define AUX_KV 2
#define AUX_W 2
#define WK 8
#define WV 8
#define AUX_GU 0

#define NPROBE 3
#define NEARV(x) (x)
constexpr int WS_EPOCH = 0;
constexpr int WS_HG = 4096;
constexpr int WS_MP = WS_HG + 4096;
constexpr int MP_S = 1280 * 8 + 640, OP_S = 768 + 128, BP_S = 4096 + 128, DP_S = 3072 + 128;
constexpr int WS_MG = WS_MP + 8 * MP_S;
constexpr int WS_QP = WS_MG + 2560 * 8;
constexpr int WS_ML = WS_QP + 32 * 8 * 1024;
constexpr int WS_CL = WS_ML + 32 * 32 * 16;
constexpr int WS_SV = (WS_CL + 40 * 128 + 16383) & ~16383;
constexpr int WS_OP = WS_SV;
constexpr int WS_BP = WS_OP + 32 * 16384;
constexpr int WS_DP = WS_BP + 10 * 16384;
constexpr int WS_PR = WS_DP + 20 * 16384;
constexpr int WS_END = WS_PR + 8 * 16384;
#define OP_AT(hd, par) (WS_OP + (hd) * 16384 + (par) * 8192 + ((hd) & 7) * 128)
#define BP_AT(cb, par) (WS_BP + (cb) * 16384 + (par) * 8192 + (cb) * 128)
#define DP_AT(jj, par) (WS_DP + (jj) * 16384 + (par) * 8192 + ((jj) & 15) * 128)
constexpr int WS_STAMP = 8 << 20;
constexpr int RED0 = 224;
constexpr u32 SENT = 0xFFFFFFFFu;
constexpr int OOB = 0x40000000;
constexpr u32 SPIN_CAP = 1u << 17;

struct Params {
  const u16 *x, *K, *V, *wq, *wo, *wg, *wu, *wd, *c, *wmod;
  u16* out;
  unsigned char* ws;
};

__device__ __forceinline__ float asf(u32 u) { return __builtin_bit_cast(float, u); }
__device__ __forceinline__ u32 asu(float f) { return __builtin_bit_cast(u32, f); }
__device__ __forceinline__ float lo_f(u32 w) { return asf(w << 16); }
__device__ __forceinline__ float hi_f(u32 w) { return asf(w & 0xffff0000u); }
__device__ __forceinline__ u32 pack2(float lo, float hi) { bf16x2 v = {(__bf16)lo, (__bf16)hi}; return __builtin_bit_cast(u32, v); }
__device__ __forceinline__ float rb(float f) { return (float)(__bf16)f; }
__device__ __forceinline__ float dot2(u32 a, u32 b, float c) { return __builtin_amdgcn_fdot2_f32_bf16(__builtin_bit_cast(bf16x2, a), __builtin_bit_cast(bf16x2, b), c, false); }
__device__ __forceinline__ u32 perm_lo(u32 a, u32 b) { return __builtin_amdgcn_perm(b, a, 0x05040100u); }
__device__ __forceinline__ u32 perm_hi(u32 a, u32 b) { return __builtin_amdgcn_perm(b, a, 0x07060302u); }
template <int CTRL> __device__ __forceinline__ float dpp(float x) { return asf(__builtin_amdgcn_mov_dpp(asu(x), CTRL, 0xf, 0xf, true)); }
constexpr int XOR1 = 0xB1, XOR2 = 0x4E, HMIR = 0x141, ROR8 = 0x128;
__device__ __forceinline__ float tr32(float a, float b) { auto s = __builtin_amdgcn_permlane32_swap(asu(a), asu(b), false, false); return asf(s[0]) + asf(s[1]); }
__device__ __forceinline__ float tr16(float a, float b) { auto s = __builtin_amdgcn_permlane16_swap(asu(a), asu(b), false, false); return asf(s[0]) + asf(s[1]); }
__device__ __forceinline__ float tr8(float a, float b, bool bit3) { const float keep = bit3 ? b : a, send = bit3 ? a : b; return keep + dpp<ROR8>(send); }
__device__ __forceinline__ float max32(float a) { auto s = __builtin_amdgcn_permlane32_swap(asu(a), asu(a), false, false); return fmaxf(asf(s[0]), asf(s[1])); }
__device__ __forceinline__ float max16(float a) { auto s = __builtin_amdgcn_permlane16_swap(asu(a), asu(a), false, false); return fmaxf(asf(s[0]), asf(s[1])); }
__device__ __forceinline__ float sum8(float a) { a += dpp<XOR1>(a); a += dpp<XOR2>(a); a += dpp<HMIR>(a); return a; }
__device__ __forceinline__ float sum_hi(float a) { a += dpp<ROR8>(a); a = tr16(a, a); return tr32(a, a); }
__device__ __forceinline__ float wave_sum(float a) { return sum_hi(sum8(a)); }
__device__ __forceinline__ float max_hi(float a) { a = fmaxf(a, dpp<ROR8>(a)); return max32(max16(a)); }
__device__ __forceinline__ float wave_max(float a) { a = fmaxf(a, dpp<XOR1>(a)); a = fmaxf(a, dpp<XOR2>(a)); a = fmaxf(a, dpp<HMIR>(a)); return max_hi(a); }
__device__ __forceinline__ float tr_reduce8(const float* v, int lane) {
  const float a0 = tr32(v[0], v[4]), a1 = tr32(v[1], v[5]), a2 = tr32(v[2], v[6]), a3 = tr32(v[3], v[7]);
  const float b0 = tr16(a0, a2), b1 = tr16(a1, a3);
  return tr8(b0, b1, (lane & 8) != 0);
}
__device__ __forceinline__ rsrc_t mk(const void* p, int bytes) { return __builtin_amdgcn_make_buffer_rsrc((void*)p, 0, bytes, 0x00020000); }
template <int AUX> __device__ __forceinline__ v4u ld4(rsrc_t r, int vo, int so) { return __builtin_amdgcn_raw_buffer_load_b128(r, vo, so, AUX); }
template <int AUX> __device__ __forceinline__ v2u ld2(rsrc_t r, int vo, int so) { return __builtin_amdgcn_raw_buffer_load_b64(r, vo, so, AUX); }
template <int AUX> __device__ __forceinline__ u32 ld1(rsrc_t r, int vo, int so) { return __builtin_amdgcn_raw_buffer_load_b32(r, vo, so, AUX); }
__device__ __forceinline__ void st4(rsrc_t r, int vo, int so, v4u v) { __builtin_amdgcn_raw_buffer_store_b128(v, r, vo, so, AUX_SC1); }
__device__ __forceinline__ void st2(rsrc_t r, int vo, int so, v2u v) { __builtin_amdgcn_raw_buffer_store_b64(v, r, vo, so, AUX_SC1); }
__device__ __forceinline__ void st1(rsrc_t r, int vo, int so, u32 v) { __builtin_amdgcn_raw_buffer_store_b32(v, r, vo, so, AUX_SC1); }
__device__ __forceinline__ u32 umax(u32 a, u32 b) { return a > b ? a : b; }
__device__ __forceinline__ u32 canon(float f) { const u32 u = asu(f); return u < 0xFFFFFFFEu ? u : 0xFFFFFFFEu; }
template <int CTRL> __device__ __forceinline__ u32 dppu(u32 x) { return __builtin_amdgcn_mov_dpp(x, CTRL, 0xf, 0xf, true); }
#define CFENCE() asm volatile("" ::: "memory")
#define LDS_WAIT() asm volatile("s_waitcnt lgkmcnt(0)" ::: "memory")
#define BARRIER() asm volatile("s_waitcnt lgkmcnt(0)\n\ts_barrier" ::: "memory")
#define SCHED() __builtin_amdgcn_sched_barrier(0)
#define KEEPV(x) asm volatile("" : "+v"(x))
__device__ __forceinline__ float frcp(float x) { return __builtin_amdgcn_rcpf(x); }
__device__ __forceinline__ float fsqrt(float x) { return __builtin_amdgcn_sqrtf(x); }
__device__ __forceinline__ float silu(float x) { return x / (1.f + expf(-x)); }
template <int N> __device__ __forceinline__ void vm_wait() { asm volatile("s_waitcnt vmcnt(%0)" :: "i"(N) : "memory"); }
__device__ __forceinline__ bool all_zero(u32 bad) { return __builtin_amdgcn_ballot_w64(bad != 0u) == 0ull; }
__device__ __forceinline__ bool lds_wait_flag(volatile LAS u32* f) {
#pragma unroll 1
  for (u32 sp = 0; sp < (SPIN_CAP << 3); ++sp) {
    if (__builtin_amdgcn_readfirstlane(*f) != 0u) { CFENCE(); return true; }
    __builtin_amdgcn_s_sleep(1);
  }
  return false;
}
#define STAMP(i) do {} while (0)
#define STAMP_DUMP() do {} while (0)
#define STAMP_DECL()

constexpr int L_KF = 0, L_GF = 4, L_PF = 8, L_QF = 12, L_T = 13, L_TF = 14, L_CNT = 15, L_WF = 16, L_NR = 20, L_HS = 24  , L_QS = 64  ,
              L_PS = 160  , L_SI = 544  , L_RED = 864  , L_GU = 1888  , L_OR = L_RED  , L_X2 = L_SI  , L_ST = L_PS  , L_H2 = L_RED  ,
              L_SPILL = 3168  , L_WORDS = L_SPILL + 8 * 15 * 256;
constexpr float LOG2E = 1.4426950408889634f;
constexpr int NG = 19, NL = 3 * NG;
#define GSTEP 30
#define WOSTEP 56
#define FREE 48
#define XBAR 1
#define WLO 3
#define KT0 60
#define KD 171
#define VD 135
#define AHEAD(sched) ((u32)__builtin_amdgcn_s_memrealtime() - t_start < (u32)(sched))
#define PACE(HI, LO, sched) do { vm_wait<HI>(); if (AHEAD(sched)) { vm_wait<(LO) + 3>(); if (AHEAD(sched)) vm_wait<LO>(); } } while (0)
#define ADELAY 14
#define ASLEEP 2
#define BDELAY 28
#define BSLEEP 2
#define DDELAY 18
#define DSLEEP 1
#define KHI 9
#define KLO 3
#define VHI 12
#define VLO 3
#define K0 1
#define CODE_KB 40
#define BRETRY 0
#define CPF 1
#define VLAG ((VHI + 2) / 3 * 3)
#define ROTM 5
#define ROTS 0
#define ROTW 0
#define WMAP 1
#define XMAP 0
#define OUTAUX 0
#define WOE 4
#define HSLEEP 2
#define WK0 3
#define WQW 16
#define WS_ 4
#define DRY 0

#define POLL(SLEEP, LOADS, CHECK) do { u32 sp_ = 0; for (;;) { CFENCE(); u32 bad = 0; LOADS; CHECK; \
    if (__builtin_expect(all_zero(bad) || ++sp_ > SPIN_CAP, 1)) break; if (SLEEP) __builtin_amdgcn_s_sleep(SLEEP); } } while (0)
#define KEEPS(x) do { x = __builtin_amdgcn_readfirstlane(x); asm volatile("" : "+s"(x)); } while (0)
#define TAG2(v) do { bad |= (v).y ^ T; KEEPV(bad); } while (0)
#define TAG4(v) do { bad |= ((v).y ^ T) | ((v).w ^ T); KEEPV(bad); } while (0)
#define XOR4(v) ((v).x ^ (v).y ^ (v).z ^ (v).w ^ T)
#define UNR _Pragma("unroll")
#define SUM8(a0, a1, a2, a3, a4, a5, a6, a7) ((((a0) + (a1)) + ((a2) + (a3))) + (((a4) + (a5)) + ((a6) + (a7))))
#define ZERO8 {0.f, 0.f, 0.f, 0.f, 0.f, 0.f, 0.f, 0.f}

__device__ __forceinline__ u32 ada_pair(u32 xp, float mb, float rden, float c0, float c1, float s0, float s1) {
  return pack2(rb(rb(rb(lo_f(xp) - mb) * rden) * c0) + s0, rb(rb(rb(hi_f(xp) - mb) * rden) * c1) + s1);
}
__device__ __forceinline__ void ln_stats(const u32* xw, float& mb, float& rden) {
  const float sm = (((lo_f(xw[0]) + hi_f(xw[0])) + (lo_f(xw[1]) + hi_f(xw[1]))) + ((lo_f(xw[2]) + hi_f(xw[2])) + (lo_f(xw[3]) + hi_f(xw[3])))) + (lo_f(xw[4]) + hi_f(xw[4]));
  const float mean = wave_sum(sm) * (1.f / D);
  float sq0 = 0.f, sq1 = 0.f;
  UNR for (int i = 0; i < 5; ++i) { const float d0 = lo_f(xw[i]) - mean, d1 = hi_f(xw[i]) - mean; sq0 = fmaf(d0, d0, sq0); sq1 = fmaf(d1, d1, sq1); }
  const float var = rb(wave_sum(sq0 + sq1) * (1.f / D));
  rden = frcp(rb(fsqrt(rb(var + 1e-5f))));
  mb = rb(mean);
}
__device__ __forceinline__ void pair_rows(v4u& a, v4u& c) { UNR for (int e = 0; e < 4; ++e) { const u32 x = a[e], y = c[e]; a[e] = perm_lo(x, y); c[e] = perm_hi(x, y); } KEEPV(a); KEEPV(c); }
__device__ __forceinline__ void mac8(float* acc, const v4u& lo, const v4u& hi, u32 ap) { UNR for (int e = 0; e < 4; ++e) { acc[2 * e] = dot2(lo[e], ap, acc[2 * e]); acc[2 * e + 1] = dot2(hi[e], ap, acc[2 * e + 1]); } }
typedef float f32x4 __attribute__((ext_vector_type(4)));
typedef __bf16 bf16x8 __attribute__((ext_vector_type(8)));
__device__ __forceinline__ f32x4 mfma(const v4u& a, const v4u& b, f32x4 c) { return __builtin_amdgcn_mfma_f32_16x16x32_bf16(__builtin_bit_cast(bf16x8, a), __builtin_bit_cast(bf16x8, b), c, 0, 0, 0); }
__device__ __forceinline__ void to_frags(v4u* r) {
  v4u f[8];
  UNR for (int e = 0; e < 4; ++e) UNR for (int k = 0; k < 4; ++k) { f[2 * e][k] = perm_lo(r[2 * k][e], r[2 * k + 1][e]); f[2 * e + 1][k] = perm_hi(r[2 * k][e], r[2 * k + 1][e]); }
  UNR for (int e = 0; e < 8; ++e) { r[e] = f[e]; KEEPV(r[e]); }
}
__device__ __forceinline__ f32x4 asf4(const v4u& v) { return __builtin_bit_cast(f32x4, v); }
__device__ __forceinline__ bool any_nan(float a) { return __builtin_amdgcn_ballot_w64(a != a) != 0ull; }
__device__ __forceinline__ float fold4(const f32x4& a) { return tr16(tr32(a.x, a.z), tr32(a.y, a.w)); }
#define GATHER(SLEEP, LOADS, SUMS, NANCHK, CHECK) do { u32 sp_ = 0; for (;;) { CFENCE(); LOADS; SCHED(); SUMS; if (__builtin_expect(!(NANCHK), 1)) break; \
    u32 bad; CHECK; if (all_zero(bad) || ++sp_ > SPIN_CAP) break; __builtin_amdgcn_s_sleep(SLEEP); } } while (0)
__device__ __forceinline__ float sum16(float a) { a = sum8(a); return a + dpp<ROR8>(a); }
__device__ __forceinline__ u32 near_mask(const v4u& g) {
  const float tot = sum8(g.z ? -(float)(int)g.x : (float)(int)g.x);
  return (u32)__builtin_amdgcn_ballot_w64(((tot < 0.f ? 1u : 0u) ^ g.z) != 0u) & 0xffu;
}
__device__ __forceinline__ u32 umax4(u32 m, const v4u& g) { return umax(umax(umax(m, g.x), g.y), umax(g.z, g.w)); }

__global__ void __launch_bounds__(NTHR, 2) fused_block(Params p) {
  __shared__ __attribute__((aligned(16))) u32 smem[L_WORDS];
  LAS u32* L = (LAS u32*)smem;
  volatile LAS u32* LV = (volatile LAS u32*)smem;
  const int tid = threadIdx.x, lane = tid & 63, wave = __builtin_amdgcn_readfirstlane(tid >> 6), b = blockIdx.x;
  const int h = (b & 7) * 4 + ((b >> 3) & 3), s = b >> 5, r8 = lane >> 3, c8 = lane & 7, w = wave & 3;
  const bool svc = s == 7, mlp = svc && (h & 3) != 3;
  const int j = 3 * (h >> 2) + (h & 3), xi = h >> 2;
  const rsrc_t ws = mk(p.ws, WS_END);
  const int l40 = lane < 40 ? lane : 39;
  u32 T = 0;
  const u32 t_start = (u32)__builtin_amdgcn_s_memrealtime();
  u64 pc0;
  int code_bytes;
  asm volatile("s_getpc_b64 %0\n.Lfb_here:\n\ts_mov_b32 %1, .Lfb_last-.Lfb_here" : "=s"(pc0), "=s"(code_bytes));
  code_bytes = code_bytes > 0 ? code_bytes : 0;
  u32 cpf = 0;
  if (CPF) {
    cpf = ld1<0>(mk((const void*)(pc0 & ~127ull), code_bytes), lane < 2 ? lane * 32768 : OOB, ((b >> 3) * 8 + wave) * 128);
    SCHED();
  }
  STAMP_DECL();
  STAMP(0);
  const rsrc_t rm = mk(p.wmod, D * 6 * D * 2), rc = mk(p.c, D * 2);
#define MKTAG(e) (__builtin_amdgcn_readfirstlane(e) * 0x9E3779B1u + 0x7F4A7C15u)
#define MOD_LD(i, rowblk, colbyte) ld4<AUX_W>(rm, r8 * 2 * 7680 + c8 * 16, (80 * (rowblk) + 16 * ((i) >> 1) + ((i) & 1)) * 7680 + (colbyte))
#define MOD_SILU(cw, sp) do { L[L_SI + wave * 40 + l40] = pack2(silu(lo_f(cw)), silu(hi_f(cw))); LDS_WAIT(); UNR for (int i = 0; i < 5; ++i) sp[i] = L[L_SI + wave * 40 + 8 * i + r8]; } while (0)
#define MOD_MAC(res, m, sp) do { float acc_[8] = ZERO8; \
    UNR for (int i = 0; i < 5; ++i) { pair_rows(m[2 * i], m[2 * i + 1]); mac8(acc_, m[2 * i], m[2 * i + 1], sp[i]); } res = tr_reduce8(acc_, lane); } while (0)
  if (wave == 5 && b < 160) {
    __builtin_amdgcn_s_setprio(3);
    const int blk = b >> 3, rblk = b & 7;
    u32 sp[5]; v4u m0[10];
    const u32 cw = ld1<0>(rc, l40 * 4, rblk * 160);
    UNR for (int i = 0; i < 10; ++i) m0[i] = MOD_LD(i, rblk, 128 * blk);
    const u32 E = ld1<AUX_SC1>(ws, 0, WS_EPOCH);
    SCHED();
    BARRIER();
    MOD_SILU(cw, sp);
    float v;
    MOD_MAC(v, m0, sp);
    const u32 Tq = MKTAG(E), vn = dppu<ROR8>(asu(v));
    if (!(lane & 8)) st4(ws, (8 * c8 + r8) * 8, WS_MP + rblk * MP_S + 512 * blk, v4u{asu(v), Tq, vn, Tq});
    __builtin_amdgcn_s_setprio(0);
    STAMP(1);
  } else if (wave == 5 && b < 192) {
    BARRIER();
    const int jc = b - 160;
    const u32 E = ld1<AUX_SC1>(ws, 0, WS_EPOCH);
    const v4u sv = {SENT, SENT, SENT, SENT};
    UNR for (int i = 0; i < 14; ++i) { if (i >= 8) vm_wait<7>(); st4(ws, lane * 16 + (i % 7) * 1024, OP_AT(jc, i / 7), sv); SCHED(); }
    UNR for (int k = 0; k < 2; ++k) {
      const int q = jc + 32 * k, so = q < 40 ? DP_AT(q >> 1, q & 1) : q < 60 ? BP_AT((q - 40) >> 1, q & 1) : OOB;
      UNR for (int i = 0; i < 4; ++i) { vm_wait<7>(); st4(ws, lane * 16 + i * 1024, so, sv); SCHED(); }
    }
    const u32 Tq = MKTAG(E);
    vm_wait<0>();
    if (lane == 0) st4(ws, 0, WS_CL + jc * 128, v4u{0u, Tq, 0u, Tq});
    STAMP(2);
  } else if (!svc && (wave == 4 || (wave == 6 && s == 6))) {
    __builtin_amdgcn_s_setprio(3);
    const int sl = wave == 4 ? s : 7;
    LAS u32* hs = L + (wave == 4 ? L_HS : L_RED);
    BARRIER();
    const rsrc_t rqw = mk(p.wq, D * QD * 2);
    v4u qw[30];
    UNR for (int i = 0; i < 30; ++i) {
      if (i >= 15 && i % 3 == 0) vm_wait<12>();
      qw[10 * (i % 3) + i / 3] = ld4<AUX_W>(rqw, r8 * 2 * 12288 + c8 * 16 + (i % 3) * 128, (80 * sl + 16 * ((i / 3) >> 1) + ((i / 3) & 1)) * 12288 + h * DH * 2);
      SCHED();
    }
    const u32 E = ld1<AUX_SC1>(ws, 0, WS_EPOCH);
    SCHED();
    UNR for (int i = 0; i < 15; ++i) pair_rows(qw[2 * i], qw[2 * i + 1]);
    T = MKTAG(E);
    if (wave == 4) { L[L_T] = T; LDS_WAIT(); LV[L_TF] = 1u; }
    STAMP(3);
    {
      v2u g;
      POLL(HSLEEP, g = ld2<AUX_SC1>(ws, l40 * 8, WS_HG + sl * 320), TAG2(g));
      hs[l40] = g.x;
      LDS_WAIT();
    }
    STAMP(4);
    {
      u32 hp[5];
      UNR for (int i = 0; i < 5; ++i) hp[i] = hs[8 * i + r8];
      u32 qv[3];
      UNR for (int t = 0; t < 3; ++t) {
        float acc[8] = ZERO8;
        UNR for (int i = 0; i < 5; ++i) mac8(acc, qw[10 * t + 2 * i], qw[10 * t + 2 * i + 1], hp[i]);
        qv[t] = asu(tr_reduce8(acc, lane));
      }
      st4(ws, lane * 16, WS_QP + (h * 8 + sl) * 1024, v4u{qv[0], qv[1], qv[2], qv[0] ^ qv[1] ^ qv[2] ^ T});
    }
    __builtin_amdgcn_s_setprio(0);
    STAMP(5);
  } else if (svc && !mlp && wave == 0) {
    const rsrc_t rx = mk(p.x, D * 2);
    u32 xw[5];
    UNR for (int i = 0; i < 5; ++i) xw[i] = ld1<0>(rx, lane * 4 + i * 256, 0);
    const u32 xme = ld1<0>(rx, l40 * 4, xi * 160);
    const u32 E = ld1<AUX_SC1>(ws, 0, WS_EPOCH);
    SCHED();
    BARRIER();
    float mb, rden;
    ln_stats(xw, mb, rden);
    T = MKTAG(E);
    v4u g[16];
    POLL(0, UNR for (int q = 0; q < 16; ++q) g[q] = ld4<AUX_SC1>(ws, l40 * 16, WS_MP + (q & 7) * MP_S + ((q >> 3) * 640 + 80 * xi) * 8),
         UNR for (int q = 0; q < 16; ++q) TAG4(g[q]));
    float v[4];
    UNR for (int k = 0; k < 4; ++k) { const int o = 8 * (k >> 1), e = 2 * (k & 1);
      v[k] = rb(SUM8(asf(g[o][e]), asf(g[o + 1][e]), asf(g[o + 2][e]), asf(g[o + 3][e]), asf(g[o + 4][e]), asf(g[o + 5][e]), asf(g[o + 6][e]), asf(g[o + 7][e]))); }
    const u32 hp = ada_pair(xme, mb, rden, rb(1.f + v[2]), rb(1.f + v[3]), v[0], v[1]), hn = dppu<XOR1>(hp);
    if (lane < 40 && !(lane & 1)) st4(ws, lane * 8, WS_HG + xi * 320, v4u{hp, T, hn, T});
    LV[L_QF] = 1u;
    STAMP(1);
  } else {
    if (wave == 7 && lane < 24) L[lane] = 0u;
    BARRIER();
    if (svc && !mlp && wave == 1) {
      const u32 E = ld1<AUX_SC1>(ws, 0, WS_EPOCH);
      if (lane < 2 * NPROBE) st1(ws, (lane & 1) * 8192 + (lane >> 1) * 640, WS_PR + xi * 16384, 0u);
      vm_wait<0>();
      u32 mn0 = ~0u, mn1 = ~0u;
#pragma unroll 1
      for (int i = 0; i < 2 * NPROBE; ++i) {
        const u32 c0 = (u32)__builtin_amdgcn_s_memtime();
        u32 g = ld1<AUX_SC1>(ws, WS_PR + xi * 16384 + (i & 1) * 8192 + (i >> 1) * 640, 0);
        asm volatile("s_waitcnt vmcnt(0)" : "+v"(g) :: "memory");
        const u32 dt = (u32)__builtin_amdgcn_s_memtime() - c0;
        if (i & 1) mn1 = dt < mn1 ? dt : mn1; else mn0 = dt < mn0 ? dt : mn0;
      }
      int df = (int)mn1 - (int)mn0;
      df = df > 400 ? 400 : df < -400 ? -400 : df;
      const u32 Tq = MKTAG(E), half = (__builtin_amdgcn_s_getreg(20 | (3 << 11)) >> 2) & 1u;
      if (lane == 0) st4(ws, 0, WS_CL + (32 + xi) * 128, v4u{(u32)df, Tq, half, Tq});
      LV[L_TF] = 1u;
      STAMP(4);
    }
  }
  const int wid = 4 * s + w;
  const bool odd = XMAP ? wid >= 20 : (3 - w) * 7 + s < 8;
  const int kvoff = r8 * 384 + c8 * 16, kvbase = (h * 512 + wid) * 3072;
  const int lastbase = odd ? (h * 512 + 504 + (XMAP ? wid - 20 : (3 - w) * 7 + s)) * 3072 : OOB;
  const int rot = (h * ROTM + s * ROTS + w * ROTW) % 18;
#define GOFF(g) (kvbase + (((g) + rot >= 18) ? (g) + rot - 18 : (g) + rot) * (28 * 3072))
  LAS v4u* spill = (LAS v4u*)(L + L_SPILL + wave * 15 * 256) + lane;
  if (!svc && wave >= 4) {
    if (!lds_wait_flag(LV + L_KF + w)) return;
    asm volatile("" :: "v"(cpf));
    STAMP(7);
    SCHED();
    const rsrc_t rv = mk(p.V, H * SK * DH * 2);
    v4u vr[42];
#define VR(i) vr[(i) % 42]
#define VSLOT(i) ((i) < 3 ? (i) : (i) >= 51 ? (i) : 3 + 6 * (((i) - 3) / 6) + 2 * (((i) - 3) % 3) + ((((i) - 3) / 3) & 1))
#define VISSUE(i) do { if ((i) % 3 == 0 && (i) >= 3 && (i) < FREE) PACE(VHI, VLO, KT0 + (57 * KD + (i) * VD) / 10); \
      VR(VSLOT(i)) = (i) < 3 ? ld4<AUX_KV>(rv, kvoff + (i) * 128, lastbase) \
                      : ld4<AUX_KV>(rv, kvoff + (((VSLOT(i) + 3) % 6) >> 1) * 128, GOFF(2 * ((VSLOT(i) - 3) / 6) + ((VSLOT(i) + 1) & 1))); SCHED(); \
      if ((i) < FREE && (i) >= 11 + VLAG && ((i) - 11 - VLAG) % 6 == 0) { UNR for (int k = 0; k < 3; ++k) pair_rows(VR((i) - 8 - VLAG + 2 * k), VR((i) - 7 - VLAG + 2 * k)); } \
      if ((i) == 17 + VLAG) { UNR for (int k = 0; k < 15; ++k) spill[64 * k] = VR(k); } \
      if ((i) == GSTEP) LV[L_GF + w] = 1u; \
      if ((i) == WOSTEP) LV[L_WF + w] = 1u; SCHED(); } while (0)
    static_assert(FREE == 48, "pairing / spill points assume this");
    UNR for (int i = 0; i < 30; ++i) VISSUE(i);
    UNR for (int i = 30; i < NL; ++i) VISSUE(i);
    STAMP(8);
    if (!lds_wait_flag(LV + L_PF + w)) return;
    STAMP(9);
    u32 pp[12];
    UNR for (int k = 0; k < 3; ++k) { const v4u t4 = *(const LAS v4u*)(L + L_PS + (w * 8 + r8) * 12 + 4 * k); UNR for (int e = 0; e < 4; ++e) pp[4 * k + e] = t4[e]; }
    int opso = OP_AT(h, __builtin_amdgcn_readfirstlane(L[L_NR])) + s * OP_S, opvo = lane < 48 ? lane * 16 : OOB;
    KEEPS(opso); KEEPV(opvo);
    float acc[24];
    UNR for (int i = 0; i < 24; ++i) acc[i] = 0.f;
    constexpr int LASTP = 8 + 6 * ((FREE - 1 - 11 - VLAG) / 6);
#define PV_T(i, t) do { if (4 + 6 * (i) + 2 * (t) > LASTP) pair_rows(VR(3 + 6 * (i) + 2 * (t)), VR(4 + 6 * (i) + 2 * (t))); \
      mac8(acc + 8 * (t), VR(3 + 6 * (i) + 2 * (t)), VR(4 + 6 * (i) + 2 * (t)), pp[i]); } while (0)
#define PV(i) do { PV_T(i, 0); PV_T(i, 1); PV_T(i, 2); SCHED(); } while (0)
    PV(2); PV(3);
    {
      v4u sv[9];
      UNR for (int k = 0; k < 9; ++k) sv[k] = spill[64 * k];
      SCHED();
      PV(4);
      UNR for (int t = 0; t < 3; ++t) {
        UNR for (int e = 0; e < 4; ++e) { acc[8 * t + 2 * e] = dot2(sv[t][e] & 0xffffu, pp[9], acc[8 * t + 2 * e]); acc[8 * t + 2 * e + 1] = dot2(sv[t][e] & 0xffff0000u, pp[10], acc[8 * t + 2 * e + 1]); }
        mac8(acc + 8 * t, sv[3 + 2 * t], sv[4 + 2 * t], pp[0]);
      }
      SCHED();
      UNR for (int k = 0; k < 6; ++k) sv[k] = spill[64 * (9 + k)];
      SCHED();
      PV(5);
      UNR for (int t = 0; t < 3; ++t) mac8(acc + 8 * t, sv[2 * t], sv[2 * t + 1], pp[1]);
      SCHED();
    }
    PV(6);
    STAMP(10);
    PV(7);
    PV_T(8, 0);
    L[L_OR + w * 192 + 8 * c8 + r8] = asu(tr_reduce8(acc, lane));
    SCHED();
    PV_T(8, 1);
    L[L_OR + w * 192 + 64 + 8 * c8 + r8] = asu(tr_reduce8(acc + 8, lane));
    SCHED();
    PV_T(8, 2);
    const float o2 = tr_reduce8(acc + 16, lane);
    L[L_OR + w * 192 + 128 + 8 * c8 + r8] = asu(o2);
    STAMP(11);
    BARRIER();
    if (w == 0) {
      const v4u t0 = *(const LAS v4u*)(L + L_OR + 4 * lane), t1 = *(const LAS v4u*)(L + L_OR + 192 + 4 * lane), t2 = *(const LAS v4u*)(L + L_OR + 384 + 4 * lane), t3 = *(const LAS v4u*)(L + L_OR + 576 + 4 * lane);
      v4u r;
      UNR for (int e = 0; e < 4; ++e) r[e] = canon((asf(t0[e]) + asf(t1[e])) + (asf(t2[e]) + asf(t3[e])));
      st4(ws, opvo, opso, r);
    }
    STAMP(12);
    STAMP_DUMP();
    return;
  }
  if (!svc) {
    const rsrc_t rk = mk(p.K, H * SK * DH * 2);
    v4u kr[45];
#define KR(i) kr[(i) % 45]
#define KISSUE(i) do { if ((i) % 3 == 0) { if ((i) >= 9) PACE(KHI, KLO, KT0 + (i) * KD / 10); else if ((i) >= 3) vm_wait<K0>(); } \
      KR(i) = ld4<AUX_KV>(rk, kvoff + ((i) % 3) * 128, (i) < 54 ? GOFF((i) / 3) : lastbase); SCHED(); \
      if ((i) == 29) { UNR for (int k = 0; k < 12; ++k) spill[64 * k] = KR(k); SCHED(); } } while (0)
    UNR for (int i = 0; i < NL; ++i) KISSUE(i);
    asm volatile("" :: "v"(cpf));
    LV[L_KF + w] = 1u;
    STAMP(1);
    if (!lds_wait_flag(LV + L_TF)) return;
    T = __builtin_amdgcn_readfirstlane(L[L_T]);
    if (w == 0) {
      v4u g[8];
      POLL(4, UNR for (int q = 0; q < 8; ++q) g[q] = ld4<AUX_SC1>(ws, lane * 16, WS_QP + (h * 8 + q) * 1024),
           UNR for (int i = 0; i < 8; ++i) { bad |= XOR4(g[i]); KEEPV(bad); });
      LAS u16* qs = (LAS u16*)(L + L_QS);
      UNR for (int t = 0; t < 3; ++t) {
        const float q = SUM8(asf(g[0][t]), asf(g[1][t]), asf(g[2][t]), asf(g[3][t]), asf(g[4][t]), asf(g[5][t]), asf(g[6][t]), asf(g[7][t]));
        qs[64 * t + 8 * c8 + r8] = (u16)pack2(q, q);
      }
      LDS_WAIT();
      LV[L_QF] = 1u;
    } else if (!lds_wait_flag(LV + L_QF)) return;
    CFENCE();
    STAMP(2);
    v4u qp[3];
    float sc[NG];
    UNR for (int t = 0; t < 3; ++t) qp[t] = *(const LAS v4u*)(L + L_QS + 32 * t + 4 * c8);
#define QK(g, k0, k1, k2) do { float a_ = 0.f; UNR for (int e = 0; e < 4; ++e) a_ = dot2((k0)[e], qp[0][e], a_); UNR for (int e = 0; e < 4; ++e) a_ = dot2((k1)[e], qp[1][e], a_); \
      UNR for (int e = 0; e < 4; ++e) a_ = dot2((k2)[e], qp[2][e], a_); sc[g] = rb(rb(sum8(a_)) * 0.07216878364870322f); } while (0)
    UNR for (int g = 4; g < NG; ++g) QK(g, KR(3 * g), KR(3 * g + 1), KR(3 * g + 2));
    SCHED();
    UNR for (int g = 0; g < 4; ++g) { const v4u k0 = spill[64 * (3 * g)], k1 = spill[64 * (3 * g + 1)], k2 = spill[64 * (3 * g + 2)]; QK(g, k0, k1, k2); }
    if (!odd) sc[18] = -INFINITY;
    float m = sc[0];
    UNR for (int g = 1; g < NG; ++g) m = fmaxf(m, sc[g]);
    m = max_hi(m);
    float l = 0.f;
    const float msub = m == -INFINITY ? 0.f : m;
    UNR for (int g = 0; g < NG; ++g) l += __builtin_amdgcn_exp2f((sc[g] - msub) * LOG2E);
    l = sum_hi(l);
    if (lane == 0) st4(ws, 0, WS_ML + (h * 32 + wid) * 16, v4u{asu(m), T, asu(l), T});
    STAMP(4);
    if (!lds_wait_flag(LV + L_GF + w)) return;
    STAMP(5);
    float M, Ls;
    int par;
    {
      const int l32 = lane & 31;
      const int vo = lane < 32 ? WS_ML + h * 512 + (l32 < 28 ? l32 : 0) * 16 : WS_CL + l32 * 128;
      v4u g, pr;
      POLL(8, g = ld4<AUX_SC1>(ws, vo, 0); pr = ld4<AUX_SC1>(ws, c8 * 128, WS_CL + 32 * 128), TAG4(g); TAG4(pr));
      par = NEARV(near_mask(pr)) >> (b & 7) & 1;
      L[L_NR] = par;
      const float mm = lane < 28 ? asf(g.x) : -INFINITY;
      M = wave_max(mm);
      Ls = wave_sum(lane < 28 ? asf(g.z) * __builtin_amdgcn_exp2f((mm - M) * LOG2E) : 0.f);
    }
    {
      const float inv = 1.f / Ls;
      v4u pp[3];
      UNR for (int i = 0; i < 9; ++i)
        pp[i >> 2][i & 3] = pack2(__builtin_amdgcn_exp2f((sc[2 * i] - M) * LOG2E) * inv, __builtin_amdgcn_exp2f((sc[2 * i + 1] - M) * LOG2E) * inv);
      const float pl = __builtin_amdgcn_exp2f((sc[18] - M) * LOG2E) * inv;
      pp[2][1] = pack2(pl, 0.f); pp[2][2] = pack2(0.f, pl); pp[2][3] = 0u;
      if (c8 == 0) { UNR for (int k = 0; k < 3; ++k) *(LAS v4u*)(L + L_PS + (w * 8 + r8) * 12 + 4 * k) = pp[k]; }
      LDS_WAIT();
      LV[L_PF + w] = 1u;
    }
    STAMP(6);
    const int li = b >> 3;
    if (li >= 20) { BARRIER(); STAMP_DUMP(); return; }
    const int rg = 2 * (b & 7) + (li >= 10), cb = li >= 10 ? li - 10 : li, hh = 2 * rg + (w >> 1), d0 = 96 * (w & 1);
    v4u ow[12];
    const rsrc_t ro = mk(p.wo, QD * D * 2);
    UNR for (int i = 0; i < WOE; ++i) ow[i] = ld4<AUX_W>(ro, r8 * 12 * 1280 + c8 * 16, (hh * DH + d0 + i) * 1280 + cb * 128);
    SCHED();
    UNR for (int k = 0; k < 4; ++k) if (!lds_wait_flag(LV + L_WF + k)) return;
    STAMP(7);
    UNR for (int i = WOE; i < 12; ++i) ow[i] = ld4<AUX_W>(ro, r8 * 12 * 1280 + c8 * 16, (hh * DH + d0 + i) * 1280 + cb * 128);
    SCHED();
    int vo = c8 < 7 ? c8 * OP_S + r8 * 48 : OOB, so = OP_AT(hh, par) + d0 * 4, bvo = lane < 32 ? (lane & 15) * 16 + (lane >> 4) * 8192 : OOB, bso = BP_AT(cb, 0) + rg * 256;
    LAS u32* lw = L + L_GU + w * 64 + 8 * c8 + r8;
    const LAS v4u* lr = (const LAS v4u*)(L + L_GU) + (lane & 15);
    KEEPV(vo); KEEPS(so); KEEPV(bvo); KEEPS(bso); KEEPV(lw); KEEPV(lr);
    BARRIER();
    STAMP(8);
    __builtin_amdgcn_s_sleep(ADELAY);
    {
      v4u g[3];
      UNR for (int k = 0; k < 3; ++k) g[k] = ld4<AUX_SC1>(ws, vo + 16 * k, so);
      SCHED();
      UNR for (int i = 0; i < 6; ++i) pair_rows(ow[2 * i], ow[2 * i + 1]);
      float acc[8] = ZERO8;
      UNR for (int e = 0; e < 8; ++e) KEEPV(acc[e]);
      SCHED();
      STAMP(9);
      if (!all_zero(umax4(umax4(umax4(0u, g[0]), g[1]), g[2]) == SENT)) {
        POLL(ASLEEP, UNR for (int k = 0; k < 3; ++k) g[k] = ld4<AUX_SC1>(ws, vo + 16 * k, so),
             bad = umax4(umax4(umax4(0u, g[0]), g[1]), g[2]) == SENT);
      }
      STAMP(10);
      float o[12];
      UNR for (int i = 0; i < 12; ++i) { o[i] = asf(g[i >> 2][i & 3]); o[i] += dpp<XOR1>(o[i]); KEEPV(o[i]); }
      UNR for (int i = 0; i < 12; ++i) { o[i] += dpp<XOR2>(o[i]); KEEPV(o[i]); }
      UNR for (int i = 0; i < 12; ++i) { o[i] += dpp<HMIR>(o[i]); KEEPV(o[i]); }
      UNR for (int i = 0; i < 6; ++i) mac8(acc, ow[2 * i], ow[2 * i + 1], pack2(o[2 * i], o[2 * i + 1]));
      *lw = asu(tr_reduce8(acc, lane));
      BARRIER();
      if (w == 0) {
        const v4u t0 = lr[0], t1 = lr[16], t2 = lr[32], t3 = lr[48];
        v4u r;
        UNR for (int e = 0; e < 4; ++e) r[e] = canon((asf(t0[e]) + asf(t1[e])) + (asf(t2[e]) + asf(t3[e])));
        st4(ws, bvo, bso, r);
      }
    }
    STAMP(11);
    STAMP_DUMP();
    return;
  }
  {
    if (!mlp && (!lds_wait_flag(LV + L_QF) || !lds_wait_flag(LV + L_TF))) return;
    {
      u32 sp[5]; v4u m0[10], m1[10];
      const u32 cw = ld1<0>(rc, l40 * 4, wave * 160);
      const u32 E = ld1<AUX_SC1>(ws, 0, WS_EPOCH);
      UNR for (int i = 0; i < 10; ++i) m0[i] = MOD_LD(i, wave, 2560 + 128 * h);
      SCHED();
      if (!mlp) { UNR for (int i = 0; i < 10; ++i) m1[i] = MOD_LD(i, wave, 2560 + 128 * (32 + xi)); }
      SCHED();
      MOD_SILU(cw, sp);
      asm volatile("" :: "v"(cpf));
      T = MKTAG(E);
      float v0;
      MOD_MAC(v0, m0, sp);
      L[L_RED + wave * 64 + 8 * c8 + r8] = asu(v0);
      if (!mlp) { MOD_MAC(v0, m1, sp); L[L_RED + 512 + wave * 64 + 8 * c8 + r8] = asu(v0); }
      BARRIER();
      if (wave == 7 || (wave == 6 && !mlp)) {
        const LAS u32* a = L + L_RED + (7 - wave) * 512 + lane;
        const u32 v = asu(rb(SUM8(asf(a[0]), asf(a[64]), asf(a[128]), asf(a[192]), asf(a[256]), asf(a[320]), asf(a[384]), asf(a[448])))), vn = dppu<XOR1>(v);
        if (!(lane & 1)) st4(ws, lane * 8, WS_MG + (wave == 6 ? 32 + xi : h) * 512, v4u{v, T, vn, T});
      }
    }
    STAMP(2);
    if (!mlp) {
      if (CODE_KB) {
        const rsrc_t rcode = mk((const void*)(pc0 & ~127ull), code_bytes);
        u32 acc = 0;
        UNR for (int i = 0; i < (CODE_KB + 7) / 8; ++i) acc |= ld4<0>(rcode, lane * 16, (wave + 8 * i) * 1024).x;
        asm volatile("" :: "v"(acc));
      }
      STAMP_DUMP(); return;
    }
    const int cg = lane & 15, rq = lane >> 4;
    v4u wf[40];
    if (wave < 4) {
      const GAS v4u* gp = (const GAS v4u*)((cg < 8 ? p.wg : p.wu) + (size_t)(160 * w + 8 * rq) * F + 64 * j + 8 * (cg & 7));
      UNR for (int i = 0; i < 40; ++i) wf[i] = __builtin_nontemporal_load(gp + (32 * (i >> 3) + (i & 7)) * (F / 8));
      SCHED();
      UNR for (int c = 0; c < 5; ++c) to_frags(wf + 8 * c);
    } else {
      const rsrc_t rd = mk(p.wd, F * D * 2);
      u32 ex[16];
      UNR for (int i = 0; i < 16; ++i) { wf[i] = ld4<AUX_W>(rd, rq * 16 * 1280 + cg * 16, (64 * j + i) * 1280 + 256 * w); ex[i] = ld1<AUX_W>(rd, rq * 16 * 1280 + cg * 4, (64 * j + i) * 1280 + 1024 + 64 * w); }
      SCHED();
      to_frags(wf); to_frags(wf + 8);
      UNR for (int c = 0; c < 2; ++c) UNR for (int k = 0; k < 4; ++k) { wf[16 + 2 * c][k] = perm_lo(ex[8 * c + 2 * k], ex[8 * c + 2 * k + 1]); wf[17 + 2 * c][k] = perm_hi(ex[8 * c + 2 * k], ex[8 * c + 2 * k + 1]); }
      UNR for (int i = 16; i < 20; ++i) KEEPV(wf[i]);
    }
    STAMP(3);
    const bool two = (wave & 6) == 4;
    const int cc = 4 * cg + rq, col0 = 64 * wave + cc, col1 = 512 + 64 * (wave & 1) + cc, colf = (j < 20 ? 32 * j : 0) + 4 * c8 + rq;
    float xv0 = lo_f(p.x[col0]), xv1 = lo_f(p.x[col1]);
    float ga0, ga1, sb0, sb1, cb0, cb1, gbv;
    u32 near;
    {
      v2u g[8]; v4u pr;
      POLL(16, UNR for (int k = 0; k < 3; ++k) { g[2 * k] = ld2<AUX_SC1>(ws, col0 * 8, WS_MG + 640 * 8 * k); g[2 * k + 1] = ld2<AUX_SC1>(ws, col1 * 8, WS_MG + 640 * 8 * k); }
           g[6] = ld2<AUX_SC1>(ws, colf * 8, WS_MG + 1920 * 8); g[7] = ld2<AUX_SC1>(ws, lane < 32 ? WS_CL + lane * 128 : WS_MG + (lane - 32) * 512, 0); pr = ld4<AUX_SC1>(ws, c8 * 128, WS_CL + 32 * 128),
           UNR for (int i = 0; i < 8; ++i) TAG2(g[i]); TAG4(pr));
      near = NEARV(near_mask(pr));
      ga0 = asf(g[0].x); ga1 = asf(g[1].x); sb0 = asf(g[2].x); sb1 = asf(g[3].x); cb0 = rb(1.f + asf(g[4].x)); cb1 = rb(1.f + asf(g[5].x)); gbv = asf(g[6].x);
    }
    const int mypar = near >> (b & 7) & 1, jm = 4 * w + (cg >> 2);
    int dvo = rq < 2 ? DP_AT(jm, near >> (jm / 3) & 1) + (cg & 3) * 32 + rq * 16 + j * 128 : OOB;
    int evo = rq == 0 ? DP_AT(16 + w, near >> ((16 + w) / 3) & 1) + cg * 8 + j * 128 : OOB;
    int bso0 = BP_AT(wave, mypar), bso1 = two ? BP_AT(4 + wave, mypar) : OOB, fso = DP_AT(j, mypar), ovo = (lane & 8) ? OOB : colf * 2, l16 = lane * 16;
    LAS u16* lx0 = (LAS u16*)(L + L_X2) + col0;
    LAS u16* lx1 = two ? (LAS u16*)(L + L_X2) + col1 : (LAS u16*)(L + L_SPILL) + lane;
    LAS u32* lst = L + L_ST + 4 * wave + (lane >> 5);
    const LAS v4u* lsr = (const LAS v4u*)(L + L_ST) + c8;
    const LAS v4u* laf = (const LAS v4u*)(L + L_H2 + 80 * w + 4 * rq);
    LAS u32* lgu = L + L_GU + (wave < 4 ? w * 128 + 8 * cg : lane);
    float cnt = (c8 & 6) == 4 ? 128.f : 64.f, rcnt = (c8 & 6) == 4 ? 1.f / 128 : 1.f / 64;
    KEEPV(dvo); KEEPV(evo); KEEPS(bso0); KEEPS(bso1); KEEPS(fso); KEEPV(ovo); KEEPV(l16); KEEPV(lx0); KEEPV(lx1); KEEPV(lst); KEEPV(lsr); KEEPV(laf); KEEPV(lgu); KEEPV(cnt); KEEPV(rcnt);
    KEEPV(xv0); KEEPV(xv1); KEEPV(cb0); KEEPV(cb1);
    const rsrc_t rout = mk(p.out, D * 2);
    STAMP(6);
    if (wave == 7) {
      u32 g;
      POLL(4, g = ld1<AUX_SC1>(ws, (lane < 7 ? lane : 0) * OP_S + 764, OP_AT(h, mypar)), bad = g == SENT);
    }
    BARRIER();
    STAMP(9);
    __builtin_amdgcn_s_sleep(BDELAY);
    STAMP(10);
    float x20, x21;
    {
      v4u g[8]; float a0, a1;
      GATHER(BSLEEP, UNR for (int i = 0; i < 8; ++i) g[i] = ld4<AUX_SC1>(ws, l16 + (i & 3) * 1024, i < 4 ? bso0 : bso1),
             a0 = fold4((asf4(g[0]) + asf4(g[1])) + (asf4(g[2]) + asf4(g[3]))); a1 = fold4((asf4(g[4]) + asf4(g[5])) + (asf4(g[6]) + asf4(g[7]))), any_nan(a0 + a1),
             bad = umax(umax(umax4(umax4(0u, g[0]), g[1]), umax4(umax4(0u, g[2]), g[3])), umax(umax4(umax4(0u, g[4]), g[5]), umax4(umax4(0u, g[6]), g[7]))) == SENT);
      STAMP(11);
      x20 = rb(xv0 + rb(ga0 * rb(a0))); x21 = rb(xv1 + rb(ga1 * rb(a1)));
      const float pv = asf(__builtin_amdgcn_readfirstlane(asu(x20)));
      const float d0 = x20 - pv, d1 = two ? x21 - pv : 0.f;
      float t = tr32(d0 + d1, fmaf(d0, d0, d1 * d1));
      t = sum16(tr16(t, t));
      lst[0] = asu(t); lst[2] = asu(pv);
      *lx0 = (u16)(asu(x20) >> 16);
      *lx1 = (u16)(asu(x21) >> 16);
      BARRIER();
    }
    STAMP(12);
    {
      const v4u st = *lsr;
      const float mk_ = fmaf(asf(st.x), rcnt, asf(st.z)), m2k = fmaf(-asf(st.x) * rcnt, asf(st.x), asf(st.y));
      const float mean = sum8(mk_ * cnt) * (1.f / D), e = mk_ - mean;
      const float var = rb(sum8(fmaf(e * cnt, e, m2k)) * (1.f / D));
      const float rden = frcp(rb(fsqrt(rb(var + 1e-5f)))), mb = rb(mean);
      u32 hp = pack2(rb(rb(rb(x20 - mb) * rden) * cb0) + sb0, rb(rb(rb(x21 - mb) * rden) * cb1) + sb1);
      KEEPV(hp);
      lx0[2 * (L_H2 - L_X2)] = (u16)hp;
      lx1[2 * (L_H2 - L_X2)] = (u16)(hp >> 16);
      BARRIER();
    }
    STAMP(7);
    if (wave < 4) {
      v4u af[5];
      UNR for (int c = 0; c < 5; ++c) af[c] = laf[4 * c];
      SCHED();
      f32x4 acc[8];
      UNR for (int e = 0; e < 8; ++e) acc[e] = f32x4{0.f, 0.f, 0.f, 0.f};
      UNR for (int c = 0; c < 5; ++c) UNR for (int e = 0; e < 8; ++e) acc[e] = mfma(af[c], wf[8 * c + e], acc[e]);
      volatile LAS u32* q = lgu;
      UNR for (int e = 0; e < 8; ++e) q[e] = asu(acc[e][0]);
    }
    BARRIER();
    STAMP(13);
    if (wave >= 4) {
      const LAS u32* a = lgu;
      const float gg = rb((asf(a[0]) + asf(a[128])) + (asf(a[256]) + asf(a[384]))), uu = rb((asf(a[64]) + asf(a[192])) + (asf(a[320]) + asf(a[448])));
      const float hh = gg * uu;
      const u32 hq = pack2(hh, dpp<XOR1>(hh));
      const v4u a0 = {dppu<0x150>(hq), dppu<0x152>(hq), dppu<0x154>(hq), dppu<0x156>(hq)}, a1 = {dppu<0x158>(hq), dppu<0x15A>(hq), dppu<0x15C>(hq), dppu<0x15E>(hq)};
      f32x4 acc[10];
      UNR for (int e = 0; e < 10; ++e) acc[e] = mfma(a0, wf[e < 8 ? e : 8 + e], f32x4{0.f, 0.f, 0.f, 0.f});
      UNR for (int e = 9; e >= 0; --e) acc[e] = mfma(a1, wf[e < 8 ? 8 + e : 10 + e], acc[e]);
      st2(ws, evo, 0, v2u{canon(acc[8][0]), canon(acc[9][0])});
      v4u r;
      UNR for (int e = 0; e < 4; ++e) r[e] = canon(rq == 0 ? acc[e][0] : acc[4 + e][0]);
      st4(ws, dvo, 0, r);
      STAMP(14);
    } else if (j < 20 && wave == 3) {
      float x2 = lo_f(((const LAS u16*)(L + L_X2))[colf]);
      KEEPV(x2);
      __builtin_amdgcn_s_sleep(DDELAY);
      v4u g[3]; float r;
      GATHER(DSLEEP, UNR for (int i = 0; i < 3; ++i) g[i] = ld4<AUX_SC1>(ws, l16 + i * 1024, fso),
             r = fold4((asf4(g[0]) + asf4(g[1])) + asf4(g[2])); r += dpp<ROR8>(r), any_nan(r),
             bad = umax4(umax4(umax4(0u, g[0]), g[1]), g[2]) == SENT);
      const float o = x2 + rb(gbv * rb(r));
      __builtin_amdgcn_raw_buffer_store_b16((u16)pack2(o, o), rout, ovo, 0, OUTAUX);
      asm volatile(".Lfb_last:");
      STAMP(15);
    } else if (j == 0 && wave == 0 && lane == 0) {
      st1(ws, 0, WS_EPOCH, T);
    }
  }
  STAMP_DUMP();
}

extern "C" void kernel_launch(void* const* d_in, const int* in_sizes, int n_in, void* d_out, int out_size, void* d_ws, size_t ws_size, hipStream_t stream) {
  Params p;
  p.x = (const u16*)d_in[0]; p.K = (const u16*)d_in[1]; p.V = (const u16*)d_in[2]; p.wq = (const u16*)d_in[3]; p.wo = (const u16*)d_in[4];
  p.wg = (const u16*)d_in[6]; p.wu = (const u16*)d_in[7]; p.wd = (const u16*)d_in[8]; p.c = (const u16*)d_in[9]; p.wmod = (const u16*)d_in[10];
  p.out = (u16*)d_out; p.ws = (unsigned char*)d_ws;
  hipLaunchKernelGGL(fused_block, dim3(NWG), dim3(NTHR), 0, stream, p);
}
```

```cpp
#include <hip/hip_runtime.h>
#include <stdint.h>
#pragma clang fp contract(off)

typedef unsigned short u16;
typedef unsigned u32;
typedef unsigned long long u64;
typedef u32 v4u __attribute__((ext_vector_type(4)));
typedef u32 v2u __attribute__((ext_vector_type(2)));
typedef __bf16 bf16x2 __attribute__((ext_vector_type(2)));
#define LAS __attribute__((address_space(3)))
#define GAS __attribute__((address_space(1)))
typedef __amdgpu_buffer_rsrc_t rsrc_t;

constexpr int D = 640, H = 32, SK = 4096, DH = 192, F = 1536, QD = H * DH;
constexpr int NWG = 256, NTHR = 512;
constexpr int AUX_NT = 2, AUX_SC1 = 16;

constexpr int WS_EPOCH = 0;
constexpr int WS_HG = 4096;
constexpr int WS_MP = WS_HG + 4096;
constexpr int MP_S = 1280 * 8 + 640, OP_S = 768 + 128;
constexpr int WS_MG = WS_MP + 8 * MP_S;
constexpr int WS_QP = WS_MG + 2560 * 8;
constexpr int WS_ML = WS_QP + 32 * 8 * 1024;
constexpr int WS_CL = WS_ML + 32 * 32 * 16;
constexpr int WS_SV = (WS_CL + 384 + 16383) & ~16383;
constexpr int WS_OP = WS_SV;
constexpr int WS_BP = WS_OP + 32 * 16384;
constexpr int WS_DP = WS_BP + 10 * 16384;
constexpr int WS_PR = WS_DP + 20 * 16384;
constexpr int WS_END = WS_PR + 8 * 16384;
#define OP_AT(hd, par) (WS_OP + (hd) * 16384 + (par) * 8192 + ((hd) & 7) * 128)
#define BP_AT(cb, par) (WS_BP + (cb) * 16384 + (par) * 8192 + (cb) * 128)
#define DP_AT(jj, par) (WS_DP + (jj) * 16384 + (par) * 8192 + ((jj) & 15) * 128)
constexpr u32 SENT = 0xFFFFFFFFu;
constexpr int OOB = 0x40000000;
constexpr u32 SPIN_CAP = 1u << 19;

struct Params {
  const u16 *x, *K, *V, *wq, *wo, *wg, *wu, *wd, *c, *wmod;
  u16* out;
  unsigned char* ws;
};

__device__ __forceinline__ float asf(u32 u) { return __builtin_bit_cast(float, u); }
__device__ __forceinline__ u32 asu(float f) { return __builtin_bit_cast(u32, f); }
__device__ __forceinline__ float lo_f(u32 w) { return asf(w << 16); }
__device__ __forceinline__ float hi_f(u32 w) { return asf(w & 0xffff0000u); }
__device__ __forceinline__ u32 pack2(float lo, float hi) { bf16x2 v = {(__bf16)lo, (__bf16)hi}; return __builtin_bit_cast(u32, v); }
__device__ __forceinline__ float rb(float f) { return (float)(__bf16)f; }
__device__ __forceinline__ float dot2(u32 a, u32 b, float c) { return __builtin_amdgcn_fdot2_f32_bf16(__builtin_bit_cast(bf16x2, a), __builtin_bit_cast(bf16x2, b), c, false); }
__device__ __forceinline__ u32 perm_lo(u32 a, u32 b) { return __builtin_amdgcn_perm(b, a, 0x05040100u); }
__device__ __forceinline__ u32 perm_hi(u32 a, u32 b) { return __builtin_amdgcn_perm(b, a, 0x07060302u); }
template <int CTRL> __device__ __forceinline__ float dpp(float x) { return asf(__builtin_amdgcn_mov_dpp(asu(x), CTRL, 0xf, 0xf, true)); }
constexpr int XOR1 = 0xB1, XOR2 = 0x4E, HMIR = 0x141, ROR8 = 0x128;
__device__ __forceinline__ float tr32(float a, float b) { auto s = __builtin_amdgcn_permlane32_swap(asu(a), asu(b), false, false); return asf(s[0]) + asf(s[1]); }
__device__ __forceinline__ float tr16(float a, float b) { auto s = __builtin_amdgcn_permlane16_swap(asu(a), asu(b), false, false); return asf(s[0]) + asf(s[1]); }
__device__ __forceinline__ float tr8(float a, float b, bool bit3) { const float keep = bit3 ? b : a, send = bit3 ? a : b; return keep + dpp<ROR8>(send); }
__device__ __forceinline__ float max32(float a) { auto s = __builtin_amdgcn_permlane32_swap(asu(a), asu(a), false, false); return fmaxf(asf(s[0]), asf(s[1])); }
__device__ __forceinline__ float max16(float a) { auto s = __builtin_amdgcn_permlane16_swap(asu(a), asu(a), false, false); return fmaxf(asf(s[0]), asf(s[1])); }
__device__ __forceinline__ float sum8(float a) { a += dpp<XOR1>(a); a += dpp<XOR2>(a); a += dpp<HMIR>(a); return a; }
__device__ __forceinline__ float sum_hi(float a) { a += dpp<ROR8>(a); a = tr16(a, a); return tr32(a, a); }
__device__ __forceinline__ float wave_sum(float a) { return sum_hi(sum8(a)); }
__device__ __forceinline__ float max_hi(float a) { a = fmaxf(a, dpp<ROR8>(a)); return max32(max16(a)); }
__device__ __forceinline__ float wave_max(float a) { a = fmaxf(a, dpp<XOR1>(a)); a = fmaxf(a, dpp<XOR2>(a)); a = fmaxf(a, dpp<HMIR>(a)); return max_hi(a); }
__device__ __forceinline__ float tr_reduce8(const float* v, int lane) {
  const float a0 = tr32(v[0], v[4]), a1 = tr32(v[1], v[5]), a2 = tr32(v[2], v[6]), a3 = tr32(v[3], v[7]);
  const float b0 = tr16(a0, a2), b1 = tr16(a1, a3);
  return tr8(b0, b1, (lane & 8) != 0);
}
__device__ __forceinline__ rsrc_t mk(const void* p, int bytes) { return __builtin_amdgcn_make_buffer_rsrc((void*)p, 0, bytes, 0x00020000); }
template <int AUX> __device__ __forceinline__ v4u ld4(rsrc_t r, int vo, int so) { return __builtin_amdgcn_raw_buffer_load_b128(r, vo, so, AUX); }
template <int AUX> __device__ __forceinline__ v2u ld2(rsrc_t r, int vo, int so) { return __builtin_amdgcn_raw_buffer_load_b64(r, vo, so, AUX); }
template <int AUX> __device__ __forceinline__ u32 ld1(rsrc_t r, int vo, int so) { return __builtin_amdgcn_raw_buffer_load_b32(r, vo, so, AUX); }
__device__ __forceinline__ void st4(rsrc_t r, int vo, int so, v4u v) { __builtin_amdgcn_raw_buffer_store_b128(v, r, vo, so, AUX_SC1); }
__device__ __forceinline__ void st2(rsrc_t r, int vo, int so, v2u v) { __builtin_amdgcn_raw_buffer_store_b64(v, r, vo, so, AUX_SC1); }
__device__ __forceinline__ void st1(rsrc_t r, int vo, int so, u32 v) { __builtin_amdgcn_raw_buffer_store_b32(v, r, vo, so, AUX_SC1); }
__device__ __forceinline__ u32 umax(u32 a, u32 b) { return a > b ? a : b; }
__device__ __forceinline__ u32 canon(float f) { const u32 u = asu(f); return u < 0xFFFFFFFEu ? u : 0xFFFFFFFEu; }
template <int CTRL> __device__ __forceinline__ u32 dppu(u32 x) { return __builtin_amdgcn_mov_dpp(x, CTRL, 0xf, 0xf, true); }
#define CFENCE() asm volatile("" ::: "memory")
#define LDS_WAIT() asm volatile("s_waitcnt lgkmcnt(0)" ::: "memory")
#define BARRIER() asm volatile("s_waitcnt lgkmcnt(0)\n\ts_barrier" ::: "memory")
#define SCHED() __builtin_amdgcn_sched_barrier(0)
#define KEEPV(x) asm volatile("" : "+v"(x))
__device__ __forceinline__ float frcp(float x) { return __builtin_amdgcn_rcpf(x); }
__device__ __forceinline__ float fsqrt(float x) { return __builtin_amdgcn_sqrtf(x); }
__device__ __forceinline__ float silu(float x) { return x / (1.f + expf(-x)); }
template <int N> __device__ __forceinline__ void vm_wait() { asm volatile("s_waitcnt vmcnt(%0)" :: "i"(N) : "memory"); }
__device__ __forceinline__ bool all_zero(u32 bad) { return __builtin_amdgcn_ballot_w64(bad != 0u) == 0ull; }
__device__ __forceinline__ bool lds_wait_flag(volatile LAS u32* f) {
#pragma unroll 1
  for (u32 sp = 0; sp < (SPIN_CAP << 5); ++sp) {
    if (__builtin_amdgcn_readfirstlane(*f) != 0u) { CFENCE(); return true; }
    __builtin_amdgcn_s_sleep(1);
  }
  return false;
}

constexpr int L_KF = 0, L_GF = 4, L_PF = 8, L_QF = 12, L_T = 13, L_TF = 14, L_CNT = 15, L_WF = 16, L_NR = 20, L_HS = 24  , L_QS = 64  ,
              L_PS = 160  , L_SI = 544  , L_RED = 864  , L_GU = 1888  , L_OR = L_RED  , L_X2 = L_SI  , L_ST = L_PS  , L_H2 = L_RED  ,
              L_SPILL = 3168  , L_WORDS = L_SPILL + 8 * 15 * 256;
constexpr float LOG2E = 1.4426950408889634f;
constexpr int NG = 19, NL = 3 * NG;
constexpr int KT0 = 135, KD = 148, VD = 134, KHI = 9, KLO = 3, VHI = 12, VLO = 3, K0 = 3, KEARLY = 36, VLAG = 12, KPRE = 6;
constexpr int FREE = 48;
constexpr int QSTEP = 53;
constexpr int GSTEP = 33;
constexpr int WOSTEP = 56;
constexpr int WOE = 4, GW = 4, MW = 4, NPROBE = 3, TH = 480;
constexpr int HSLEEP = 4, ADELAY = 14, ASLEEP = 2, BDELAY = 28, BSLEEP = 2, DDELAY = 18, DSLEEP = 1;
#define AHEAD(sched) ((u32)__builtin_amdgcn_s_memrealtime() - t_start < (u32)(sched))
#define PACE(HI, LO, sched) do { if ((HI) == 6) vm_wait<6>(); else if ((HI) == 9) vm_wait<9>(); else vm_wait<12>(); if (AHEAD(sched)) { vm_wait<(LO) + 3>(); if (AHEAD(sched)) vm_wait<LO>(); } } while (0)

#define POLL(SLEEP, LOADS, CHECK) do { u32 sp_ = 0; for (;; ++sp_) { CFENCE(); u32 bad = 0; LOADS; CHECK; if (all_zero(bad)) break; if (sp_ > SPIN_CAP) return; \
    if (SLEEP) __builtin_amdgcn_s_sleep(SLEEP); } } while (0)
#define SPOLL(SLEEP, LOADS, CHECK) do { u32 sp_ = 0; for (;;) { CFENCE(); u32 bad = 0; LOADS; CHECK; \
    if (__builtin_expect(all_zero(bad) || ++sp_ > SPIN_CAP, 1)) break; __builtin_amdgcn_s_sleep(SLEEP); } } while (0)
#define KEEPS(x) do { x = __builtin_amdgcn_readfirstlane(x); asm volatile("" : "+s"(x)); } while (0)
#define TAG2(v) do { bad |= (v).y ^ T; KEEPV(bad); } while (0)
#define TAG4(v) do { bad |= ((v).y ^ T) | ((v).w ^ T); KEEPV(bad); } while (0)
#define XOR4(v) ((v).x ^ (v).y ^ (v).z ^ (v).w ^ T)
#define UNR _Pragma("unroll")
#define SUM8(a0, a1, a2, a3, a4, a5, a6, a7) ((((a0) + (a1)) + ((a2) + (a3))) + (((a4) + (a5)) + ((a6) + (a7))))
#define ZERO8 {0.f, 0.f, 0.f, 0.f, 0.f, 0.f, 0.f, 0.f}

__device__ __forceinline__ u32 ada_pair(u32 xp, float mb, float rden, float c0, float c1, float s0, float s1) {
  return pack2(rb(rb(rb(lo_f(xp) - mb) * rden) * c0) + s0, rb(rb(rb(hi_f(xp) - mb) * rden) * c1) + s1);
}
__device__ __forceinline__ void ln_stats(const u32* xw, float& mb, float& rden) {
  const float sm = (((lo_f(xw[0]) + hi_f(xw[0])) + (lo_f(xw[1]) + hi_f(xw[1]))) + ((lo_f(xw[2]) + hi_f(xw[2])) + (lo_f(xw[3]) + hi_f(xw[3])))) + (lo_f(xw[4]) + hi_f(xw[4]));
  const float mean = wave_sum(sm) * (1.f / D);
  float sq0 = 0.f, sq1 = 0.f;
  UNR for (int i = 0; i < 5; ++i) { const float d0 = lo_f(xw[i]) - mean, d1 = hi_f(xw[i]) - mean; sq0 = fmaf(d0, d0, sq0); sq1 = fmaf(d1, d1, sq1); }
  const float var = rb(wave_sum(sq0 + sq1) * (1.f / D));
  rden = frcp(rb(fsqrt(rb(var + 1e-5f))));
  mb = rb(mean);
}
__device__ __forceinline__ void pair_rows(v4u& a, v4u& c) { UNR for (int e = 0; e < 4; ++e) { const u32 x = a[e], y = c[e]; a[e] = perm_lo(x, y); c[e] = perm_hi(x, y); } KEEPV(a); KEEPV(c); }
__device__ __forceinline__ void mac8(float* acc, const v4u& lo, const v4u& hi, u32 ap) { UNR for (int e = 0; e < 4; ++e) { acc[2 * e] = dot2(lo[e], ap, acc[2 * e]); acc[2 * e + 1] = dot2(hi[e], ap, acc[2 * e + 1]); } }
typedef float f32x4 __attribute__((ext_vector_type(4)));
typedef __bf16 bf16x8 __attribute__((ext_vector_type(8)));
__device__ __forceinline__ f32x4 mfma(const v4u& a, const v4u& b, f32x4 c) { return __builtin_amdgcn_mfma_f32_16x16x32_bf16(__builtin_bit_cast(bf16x8, a), __builtin_bit_cast(bf16x8, b), c, 0, 0, 0); }
__device__ __forceinline__ void to_frags(v4u* r) {
  v4u f[8];
  UNR for (int e = 0; e < 4; ++e) UNR for (int k = 0; k < 4; ++k) { f[2 * e][k] = perm_lo(r[2 * k][e], r[2 * k + 1][e]); f[2 * e + 1][k] = perm_hi(r[2 * k][e], r[2 * k + 1][e]); }
  UNR for (int e = 0; e < 8; ++e) { r[e] = f[e]; KEEPV(r[e]); }
}
__device__ __forceinline__ f32x4 asf4(const v4u& v) { return __builtin_bit_cast(f32x4, v); }
__device__ __forceinline__ bool any_nan(float a) { return __builtin_amdgcn_ballot_w64(a != a) != 0ull; }
__device__ __forceinline__ float fold4(const f32x4& a) { return tr16(tr32(a.x, a.z), tr32(a.y, a.w)); }
#define GATHER(SLEEP, LOADS, SUMS, NANCHK, CHECK) do { u32 sp_ = 0; for (;;) { CFENCE(); LOADS; SCHED(); SUMS; if (__builtin_expect(!(NANCHK), 1)) break; \
    u32 bad; CHECK; if (all_zero(bad) || ++sp_ > SPIN_CAP) break; __builtin_amdgcn_s_sleep(SLEEP); } } while (0)
__device__ __forceinline__ float sum16(float a) { a = sum8(a); return a + dpp<ROR8>(a); }
__device__ __forceinline__ u32 near_mask(const v4u& g) {
  const float tot = sum8(g.z ? -(float)(int)g.x : (float)(int)g.x);
  return (u32)__builtin_amdgcn_ballot_w64(((tot < 0.f ? 1u : 0u) ^ g.z) != 0u) & 0xffu;
}
__device__ __forceinline__ u32 umax4(u32 m, const v4u& g) { return umax(umax(umax(m, g.x), g.y), umax(g.z, g.w)); }

__global__ void __launch_bounds__(NTHR, 2) fused_block(Params p) {
  __shared__ __attribute__((aligned(16))) u32 smem[L_WORDS];
  LAS u32* L = (LAS u32*)smem;
  volatile LAS u32* LV = (volatile LAS u32*)smem;
  const int tid = threadIdx.x, lane = tid & 63, wave = __builtin_amdgcn_readfirstlane(tid >> 6), b = blockIdx.x;
  const int h = (b & 7) * 4 + ((b >> 3) & 3), s = b >> 5, r8 = lane >> 3, c8 = lane & 7, w = wave & 3;
  const bool svc = s == 7, mlp = svc && (h & 3) != 3;
  const int j = 3 * (h >> 2) + (h & 3), xi = h >> 2;
  const rsrc_t ws = mk(p.ws, WS_END);
  const int l40 = lane < 40 ? lane : 39;
  u32 T = 0;
  u64 pc0;
  int code_bytes;
  asm volatile("s_getpc_b64 %0\n.Lfb_here:\n\ts_mov_b32 %1, .Lfb_last-.Lfb_here" : "=s"(pc0), "=s"(code_bytes));
  code_bytes = code_bytes > 0 ? code_bytes : 0;
  u32 cpf = 0;
  {
    cpf = ld1<0>(mk((const void*)(pc0 & ~127ull), code_bytes), lane < 2 ? lane * 32768 : OOB, ((b >> 3) * 8 + wave) * 128);
  }
  const int wid = 4 * s + w;
  const bool odd = s < 4 && w >= 2;
  const int kvoff = r8 * 384 + c8 * 16, kvbase = (h * 512 + wid) * 3072;
  const int lastbase = odd ? (h * 512 + 504 + 2 * s + w - 2) * 3072 : OOB;
  const int rot = (h * 5) % 18;
#define GOFF(g) (kvbase + (((g) + rot >= 18) ? (g) + rot - 18 : (g) + rot) * (28 * 3072))
  LAS v4u* spill = (LAS v4u*)(L + L_SPILL + wave * 15 * 256) + lane;
  int kg0 = GOFF(0), kg1 = GOFF(1), kvo = kvoff, ksz = !svc && wave < 4 ? H * SK * DH * 2 : 0;
  KEEPS(kg0); KEEPS(kg1); KEEPV(kvo); KEEPS(ksz);
  SCHED();
  const rsrc_t rk = mk(p.K, ksz);
  v4u kr0[6];
  if (ksz) { UNR for (int i = 0; i < 6; ++i) kr0[i] = ld4<AUX_NT>(rk, kvo + (i % 3) * 128, i < 3 ? kg0 : kg1); }
  SCHED();
  asm volatile("" :: "s"(p.K), "s"(p.ws));
  SCHED();
  const u32 t_start = (u32)__builtin_amdgcn_s_memrealtime();
  SCHED();
  const rsrc_t rm = mk(p.wmod, D * 6 * D * 2), rc = mk(p.c, D * 2);
#define MKTAG(e) (__builtin_amdgcn_readfirstlane(e) * 0x9E3779B1u + 0x7F4A7C15u)
#define MOD_LD(i, rowblk, colbyte) ld4<AUX_NT>(rm, r8 * 2 * 7680 + c8 * 16, (80 * (rowblk) + 16 * ((i) >> 1) + ((i) & 1)) * 7680 + (colbyte))
#define MOD_SILU(cw, sp) do { L[L_SI + wave * 40 + l40] = pack2(silu(lo_f(cw)), silu(hi_f(cw))); LDS_WAIT(); UNR for (int i = 0; i < 5; ++i) sp[i] = L[L_SI + wave * 40 + 8 * i + r8]; } while (0)
#define MOD_MAC(res, m, sp) do { float acc_[8] = ZERO8; \
    UNR for (int i = 0; i < 5; ++i) { pair_rows(m[2 * i], m[2 * i + 1]); mac8(acc_, m[2 * i], m[2 * i + 1], sp[i]); } res = tr_reduce8(acc_, lane); } while (0)
  if (wave == 5 && b < 160) {
    __builtin_amdgcn_s_setprio(3);
    const int blk = b >> 3, rblk = b & 7;
    u32 sp[5]; v4u m0[10];
    const u32 cw = ld1<0>(rc, l40 * 4, rblk * 160);
    UNR for (int i = 0; i < 10; ++i) m0[i] = MOD_LD(i, rblk, 128 * blk);
    const u32 E = ld1<AUX_SC1>(ws, 0, WS_EPOCH);
    SCHED();
    BARRIER();
    MOD_SILU(cw, sp);
    float v;
    MOD_MAC(v, m0, sp);
    const u32 Tq = MKTAG(E), vn = dppu<ROR8>(asu(v));
    if (!(lane & 8)) st4(ws, (8 * c8 + r8) * 8, WS_MP + rblk * MP_S + 512 * blk, v4u{asu(v), Tq, vn, Tq});
    __builtin_amdgcn_s_setprio(0);
  } else if (wave == 5 && b < 192) {
    BARRIER();
    const int jc = b - 160;
    const u32 E = ld1<AUX_SC1>(ws, 0, WS_EPOCH);
    const v4u sv = {SENT, SENT, SENT, SENT};
    UNR for (int i = 0; i < 14; ++i) { if (i >= 8) vm_wait<7>(); st4(ws, lane * 16 + (i % 7) * 1024, OP_AT(jc, i / 7), sv); SCHED(); }
    UNR for (int k = 0; k < 2; ++k) {
      const int q = jc + 32 * k, so = q < 40 ? DP_AT(q >> 1, q & 1) : q < 60 ? BP_AT((q - 40) >> 1, q & 1) : OOB;
      UNR for (int i = 0; i < 4; ++i) { vm_wait<7>(); st4(ws, lane * 16 + i * 1024, so, sv); SCHED(); }
    }
    const u32 Tq = MKTAG(E);
    vm_wait<0>();
    if (lane == 0) st2(ws, 0, WS_CL + 128 + jc * 8, v2u{0u, Tq});
  } else if (!svc && (wave == 4 || (wave == 6 && s == 6))) {
    __builtin_amdgcn_s_setprio(3);
    const int sl = wave == 4 ? s : 7;
    LAS u32* hs = L + (wave == 4 ? L_HS : L_RED);
    BARRIER();
    const rsrc_t rqw = mk(p.wq, D * QD * 2);
    v4u qw[30];
    UNR for (int i = 0; i < 30; ++i) {
      if (i >= 15 && i % 3 == 0) vm_wait<12>();
      qw[10 * (i % 3) + i / 3] = ld4<AUX_NT>(rqw, r8 * 2 * 12288 + c8 * 16 + (i % 3) * 128, (80 * sl + 16 * ((i / 3) >> 1) + ((i / 3) & 1)) * 12288 + h * DH * 2);
      SCHED();
    }
    const u32 E = ld1<AUX_SC1>(ws, 0, WS_EPOCH);
    SCHED();
    UNR for (int i = 0; i < 15; ++i) pair_rows(qw[2 * i], qw[2 * i + 1]);
    T = MKTAG(E);
    if (wave == 4) { L[L_T] = T; LDS_WAIT(); LV[L_TF] = 1u; }
    {
      v2u g;
      while (AHEAD(TH)) __builtin_amdgcn_s_sleep(4);
      POLL(HSLEEP, g = ld2<AUX_SC1>(ws, l40 * 8, WS_HG + sl * 320), TAG2(g));
      hs[l40] = g.x;
      LDS_WAIT();
    }
    {
      u32 hp[5];
      UNR for (int i = 0; i < 5; ++i) hp[i] = hs[8 * i + r8];
      u32 qv[3];
      UNR for (int t = 0; t < 3; ++t) {
        float acc[8] = ZERO8;
        UNR for (int i = 0; i < 5; ++i) mac8(acc, qw[10 * t + 2 * i], qw[10 * t + 2 * i + 1], hp[i]);
        qv[t] = asu(tr_reduce8(acc, lane));
      }
      st4(ws, lane * 16, WS_QP + (h * 8 + sl) * 1024, v4u{qv[0], qv[1], qv[2], qv[0] ^ qv[1] ^ qv[2] ^ T});
    }
    __builtin_amdgcn_s_setprio(0);
  } else if (svc && !mlp && wave == 0) {
    const rsrc_t rx = mk(p.x, D * 2);
    u32 xw[5];
    UNR for (int i = 0; i < 5; ++i) xw[i] = ld1<0>(rx, lane * 4 + i * 256, 0);
    const u32 xme = ld1<0>(rx, l40 * 4, xi * 160);
    const u32 E = ld1<AUX_SC1>(ws, 0, WS_EPOCH);
    SCHED();
    BARRIER();
    float mb, rden;
    ln_stats(xw, mb, rden);
    T = MKTAG(E);
    v4u g[16];
    POLL(0, UNR for (int q = 0; q < 16; ++q) g[q] = ld4<AUX_SC1>(ws, l40 * 16, WS_MP + (q & 7) * MP_S + ((q >> 3) * 640 + 80 * xi) * 8),
         UNR for (int q = 0; q < 16; ++q) TAG4(g[q]));
    float v[4];
    UNR for (int k = 0; k < 4; ++k) { const int o = 8 * (k >> 1), e = 2 * (k & 1);
      v[k] = rb(SUM8(asf(g[o][e]), asf(g[o + 1][e]), asf(g[o + 2][e]), asf(g[o + 3][e]), asf(g[o + 4][e]), asf(g[o + 5][e]), asf(g[o + 6][e]), asf(g[o + 7][e]))); }
    const u32 hp = ada_pair(xme, mb, rden, rb(1.f + v[2]), rb(1.f + v[3]), v[0], v[1]), hn = dppu<XOR1>(hp);
    if (lane < 40 && !(lane & 1)) st4(ws, lane * 8, WS_HG + xi * 320, v4u{hp, T, hn, T});
    LV[L_QF] = 1u;
  } else {
    if (wave == 7 && lane < 24) L[lane] = 0u;
    BARRIER();
    if (!svc && wave == 7) {
      if (!lds_wait_flag(LV + L_TF)) return;
      T = __builtin_amdgcn_readfirstlane(L[L_T]);
      if (!lds_wait_flag(LV + L_CNT)) return;
      v4u g[8];
      POLL(8, UNR for (int q = 0; q < 8; ++q) g[q] = ld4<AUX_SC1>(ws, lane * 16, WS_QP + (h * 8 + q) * 1024),
           UNR for (int i = 0; i < 8; ++i) { bad |= XOR4(g[i]); KEEPV(bad); });
      LAS u16* qs = (LAS u16*)(L + L_QS);
      UNR for (int t = 0; t < 3; ++t) {
        const float q = SUM8(asf(g[0][t]), asf(g[1][t]), asf(g[2][t]), asf(g[3][t]), asf(g[4][t]), asf(g[5][t]), asf(g[6][t]), asf(g[7][t]));
        qs[64 * t + 8 * c8 + r8] = (u16)pack2(q, q);
      }
      LDS_WAIT();
      LV[L_QF] = 1u;
    }
    if (svc && !mlp && wave == 1) {
      const u32 E = ld1<AUX_SC1>(ws, 0, WS_EPOCH);
      if (lane < 2 * NPROBE) st1(ws, (lane & 1) * 8192 + (lane >> 1) * 640, WS_PR + xi * 16384, 0u);
      vm_wait<0>();
      u32 mn0 = ~0u, mn1 = ~0u;
#pragma unroll 1
      for (int i = 0; i < 2 * NPROBE; ++i) {
        const u32 c0 = (u32)__builtin_amdgcn_s_memtime();
        u32 g = ld1<AUX_SC1>(ws, WS_PR + xi * 16384 + (i & 1) * 8192 + (i >> 1) * 640, 0);
        asm volatile("s_waitcnt vmcnt(0)" : "+v"(g) :: "memory");
        const u32 dt = (u32)__builtin_amdgcn_s_memtime() - c0;
        if (i & 1) mn1 = dt < mn1 ? dt : mn1; else mn0 = dt < mn0 ? dt : mn0;
      }
      int df = (int)mn1 - (int)mn0;
      df = df > 400 ? 400 : df < -400 ? -400 : df;
      const u32 Tq = MKTAG(E), half = (__builtin_amdgcn_s_getreg(20 | (3 << 11)) >> 2) & 1u;
      if (lane == 0) st4(ws, 0, WS_CL + xi * 16, v4u{(u32)df, Tq, half, Tq});
      LV[L_TF] = 1u;
    }
  }
  if (!svc && wave >= 4) {
    if (!lds_wait_flag(LV + L_KF + w)) return;
    asm volatile("" :: "v"(cpf));
    SCHED();
    const rsrc_t rv = mk(p.V, H * SK * DH * 2);
    v4u vr[42];
#define VR(i) vr[(i) % 42]
#define VSLOT(i) ((i) < 3 ? (i) : (i) >= 51 ? (i) : 3 + 6 * (((i) - 3) / 6) + 2 * (((i) - 3) % 3) + ((((i) - 3) / 3) & 1))
#define VISSUE(i) do { if ((i) % 3 == 0 && (i) >= 3 && (i) < FREE) PACE(VHI, VLO, KT0 + (57 * KD + (i) * VD) / 10); \
      VR(VSLOT(i)) = (i) < 3 ? ld4<AUX_NT>(rv, kvoff + (i) * 128, lastbase) \
                      : ld4<AUX_NT>(rv, kvoff + (((VSLOT(i) + 3) % 6) >> 1) * 128, GOFF(2 * ((VSLOT(i) - 3) / 6) + ((VSLOT(i) + 1) & 1))); SCHED(); \
      if ((i) < FREE && (i) >= 11 + VLAG && ((i) - 11 - VLAG) % 6 == 0) { UNR for (int k = 0; k < 3; ++k) pair_rows(VR((i) - 8 - VLAG + 2 * k), VR((i) - 7 - VLAG + 2 * k)); } \
      if ((i) == 17 + VLAG) { UNR for (int k = 0; k < 15; ++k) spill[64 * k] = VR(k); } \
      if ((i) == GSTEP) LV[L_GF + w] = 1u; \
      if ((i) == WOSTEP) LV[L_WF + w] = 1u; SCHED(); } while (0)
    static_assert(FREE == 48, "pairing / spill points assume this");
    UNR for (int i = 0; i < 30; ++i) VISSUE(i);
    UNR for (int i = 30; i < NL; ++i) VISSUE(i);
    if (!lds_wait_flag(LV + L_PF + w)) return;
    u32 pp[12];
    UNR for (int k = 0; k < 3; ++k) { const v4u t4 = *(const LAS v4u*)(L + L_PS + (w * 8 + r8) * 12 + 4 * k); UNR for (int e = 0; e < 4; ++e) pp[4 * k + e] = t4[e]; }
    int opso = OP_AT(h, __builtin_amdgcn_readfirstlane(L[L_NR])) + s * OP_S, opvo = lane < 48 ? lane * 16 : OOB;
    KEEPS(opso); KEEPV(opvo);
    float acc[24];
    UNR for (int i = 0; i < 24; ++i) acc[i] = 0.f;
    constexpr int LASTP = 8 + 6 * ((FREE - 1 - 11 - VLAG) / 6);
#define PV_T(i, t) do { if (4 + 6 * (i) + 2 * (t) > LASTP) pair_rows(VR(3 + 6 * (i) + 2 * (t)), VR(4 + 6 * (i) + 2 * (t))); \
      mac8(acc + 8 * (t), VR(3 + 6 * (i) + 2 * (t)), VR(4 + 6 * (i) + 2 * (t)), pp[i]); } while (0)
#define PV(i) do { PV_T(i, 0); PV_T(i, 1); PV_T(i, 2); SCHED(); } while (0)
    PV(2); PV(3);
    {
      v4u sv[9];
      UNR for (int k = 0; k < 9; ++k) sv[k] = spill[64 * k];
      SCHED();
      PV(4);
      UNR for (int t = 0; t < 3; ++t) {
        UNR for (int e = 0; e < 4; ++e) { acc[8 * t + 2 * e] = dot2(sv[t][e] & 0xffffu, pp[9], acc[8 * t + 2 * e]); acc[8 * t + 2 * e + 1] = dot2(sv[t][e] & 0xffff0000u, pp[10], acc[8 * t + 2 * e + 1]); }
        mac8(acc + 8 * t, sv[3 + 2 * t], sv[4 + 2 * t], pp[0]);
      }
      SCHED();
      UNR for (int k = 0; k < 6; ++k) sv[k] = spill[64 * (9 + k)];
      SCHED();
      PV(5);
      UNR for (int t = 0; t < 3; ++t) mac8(acc + 8 * t, sv[2 * t], sv[2 * t + 1], pp[1]);
      SCHED();
    }
    PV(6);
    PV(7);
    PV_T(8, 0);
    L[L_OR + w * 192 + 8 * c8 + r8] = asu(tr_reduce8(acc, lane));
    SCHED();
    PV_T(8, 1);
    L[L_OR + w * 192 + 64 + 8 * c8 + r8] = asu(tr_reduce8(acc + 8, lane));
    SCHED();
    PV_T(8, 2);
    const float o2 = tr_reduce8(acc + 16, lane);
    L[L_OR + w * 192 + 128 + 8 * c8 + r8] = asu(o2);
    BARRIER();
    if (w == 0) {
      const v4u t0 = *(const LAS v4u*)(L + L_OR + 4 * lane), t1 = *(const LAS v4u*)(L + L_OR + 192 + 4 * lane), t2 = *(const LAS v4u*)(L + L_OR + 384 + 4 * lane), t3 = *(const LAS v4u*)(L + L_OR + 576 + 4 * lane);
      v4u r;
      UNR for (int e = 0; e < 4; ++e) r[e] = canon((asf(t0[e]) + asf(t1[e])) + (asf(t2[e]) + asf(t3[e])));
      st4(ws, opvo, opso, r);
    }
    return;
  }
  if (!svc) {
    v4u kr[45];
#define KR(i) kr[(i) % 45]
#define KISSUE(i) do { if ((i) % 3 == 0) { if ((i) >= 9) PACE((i) < KEARLY ? 6 : KHI, KLO, KT0 + (i) * KD / 10); else if ((i) >= 3) vm_wait<K0>(); } \
      KR(i) = ld4<AUX_NT>(rk, kvoff + ((i) % 3) * 128, (i) < 54 ? GOFF((i) / 3) : lastbase); SCHED(); \
      if ((i) == 29) { UNR for (int k = 0; k < 12; ++k) spill[64 * k] = KR(k); SCHED(); } \
      if ((i) == QSTEP && w == 3) LV[L_CNT] = 1u; } while (0)
    UNR for (int i = 0; i < KPRE; ++i) kr[i] = kr0[i];
    UNR for (int i = KPRE; i < NL; ++i) KISSUE(i);
    asm volatile("" :: "v"(cpf));
    LV[L_KF + w] = 1u;
    if (!lds_wait_flag(LV + L_QF)) return;
    T = __builtin_amdgcn_readfirstlane(L[L_T]);
    CFENCE();
    v4u qp[3];
    float sc[NG];
    UNR for (int t = 0; t < 3; ++t) qp[t] = *(const LAS v4u*)(L + L_QS + 32 * t + 4 * c8);
#define QK(g, k0, k1, k2) do { float a_ = 0.f; UNR for (int e = 0; e < 4; ++e) a_ = dot2((k0)[e], qp[0][e], a_); UNR for (int e = 0; e < 4; ++e) a_ = dot2((k1)[e], qp[1][e], a_); \
      UNR for (int e = 0; e < 4; ++e) a_ = dot2((k2)[e], qp[2][e], a_); sc[g] = rb(rb(sum8(a_)) * 0.07216878364870322f); } while (0)
    UNR for (int g = 0; g < 4; ++g) { const v4u k0 = spill[64 * (3 * g)], k1 = spill[64 * (3 * g + 1)], k2 = spill[64 * (3 * g + 2)]; QK(g, k0, k1, k2); }
    UNR for (int g = 4; g < 14; ++g) QK(g, KR(3 * g), KR(3 * g + 1), KR(3 * g + 2));
    SCHED();
    UNR for (int g = 14; g < NG; ++g) QK(g, KR(3 * g), KR(3 * g + 1), KR(3 * g + 2));
    if (!odd) sc[18] = -INFINITY;
    float m = sc[0];
    UNR for (int g = 1; g < NG; ++g) m = fmaxf(m, sc[g]);
    m = max_hi(m);
    float l = 0.f;
    const float msub = m == -INFINITY ? 0.f : m;
    UNR for (int g = 0; g < NG; ++g) l += __builtin_amdgcn_exp2f((sc[g] - msub) * LOG2E);
    l = sum_hi(l);
    if (lane == 0) st4(ws, 0, WS_ML + (h * 32 + wid) * 16, v4u{asu(m), T, asu(l), T});
    if (!lds_wait_flag(LV + L_GF + w)) return;
    float M, Ls;
    int par;
    {
      const bool st = lane >= 32 && lane < 60;
      const int vo = st ? WS_ML + h * 512 + (lane - 32) * 16 : WS_CL + (lane < 24 ? lane : 0) * 16;
      v4u g;
      POLL(8, g = ld4<AUX_SC1>(ws, vo, 0), TAG4(g));
      par = near_mask(g) >> (b & 7) & 1;
      L[L_NR] = par;
      const float mm = st ? asf(g.x) : -INFINITY;
      M = wave_max(mm);
      Ls = wave_sum(st ? asf(g.z) * __builtin_amdgcn_exp2f((mm - M) * LOG2E) : 0.f);
    }
    {
      const float inv = 1.f / Ls;
      v4u pp[3];
      UNR for (int i = 0; i < 9; ++i)
        pp[i >> 2][i & 3] = pack2(__builtin_amdgcn_exp2f((sc[2 * i] - M) * LOG2E) * inv, __builtin_amdgcn_exp2f((sc[2 * i + 1] - M) * LOG2E) * inv);
      const float pl = __builtin_amdgcn_exp2f((sc[18] - M) * LOG2E) * inv;
      pp[2][1] = pack2(pl, 0.f); pp[2][2] = pack2(0.f, pl); pp[2][3] = 0u;
      if (c8 == 0) { UNR for (int k = 0; k < 3; ++k) *(LAS v4u*)(L + L_PS + (w * 8 + r8) * 12 + 4 * k) = pp[k]; }
      LDS_WAIT();
      LV[L_PF + w] = 1u;
    }
    const int li = b >> 3;
    if (li >= 20) { BARRIER(); return; }
    const int rg = 2 * (b & 7) + (li >= 10), cb = li >= 10 ? li - 10 : li, hh = 2 * rg + (w >> 1), d0 = 96 * (w & 1);
    v4u ow[12];
    const rsrc_t ro = mk(p.wo, QD * D * 2);
    UNR for (int i = 0; i < WOE; ++i) ow[i] = ld4<AUX_NT>(ro, r8 * 12 * 1280 + c8 * 16, (hh * DH + d0 + i) * 1280 + cb * 128);
    SCHED();
    UNR for (int k = 0; k < 4; ++k) if (!lds_wait_flag(LV + L_WF + k)) return;
    UNR for (int i = WOE; i < 12; ++i) ow[i] = ld4<AUX_NT>(ro, r8 * 12 * 1280 + c8 * 16, (hh * DH + d0 + i) * 1280 + cb * 128);
    SCHED();
    int vo = c8 < 7 ? c8 * OP_S + r8 * 48 : OOB, so = OP_AT(hh, par) + d0 * 4, bvo = lane < 32 ? (lane & 15) * 16 + (lane >> 4) * 8192 : OOB, bso = BP_AT(cb, 0) + rg * 256;
    LAS u32* lw = L + L_GU + w * 64 + 8 * c8 + r8;
    const LAS v4u* lr = (const LAS v4u*)(L + L_GU) + (lane & 15);
    KEEPV(vo); KEEPS(so); KEEPV(bvo); KEEPS(bso); KEEPV(lw); KEEPV(lr);
    BARRIER();
    __builtin_amdgcn_s_sleep(ADELAY);
    {
      v4u g[3];
      UNR for (int k = 0; k < 3; ++k) g[k] = ld4<AUX_SC1>(ws, vo + 16 * k, so);
      SCHED();
      UNR for (int i = 0; i < 6; ++i) pair_rows(ow[2 * i], ow[2 * i + 1]);
      float acc[8] = ZERO8;
      UNR for (int e = 0; e < 8; ++e) KEEPV(acc[e]);
      SCHED();
      if (!all_zero(umax4(umax4(umax4(0u, g[0]), g[1]), g[2]) == SENT)) {
        SPOLL(ASLEEP, UNR for (int k = 0; k < 3; ++k) g[k] = ld4<AUX_SC1>(ws, vo + 16 * k, so),
             bad = umax4(umax4(umax4(0u, g[0]), g[1]), g[2]) == SENT);
      }
      float o[12];
      UNR for (int i = 0; i < 12; ++i) { o[i] = asf(g[i >> 2][i & 3]); o[i] += dpp<XOR1>(o[i]); KEEPV(o[i]); }
      UNR for (int i = 0; i < 12; ++i) { o[i] += dpp<XOR2>(o[i]); KEEPV(o[i]); }
      UNR for (int i = 0; i < 12; ++i) { o[i] += dpp<HMIR>(o[i]); KEEPV(o[i]); }
      UNR for (int i = 0; i < 6; ++i) mac8(acc, ow[2 * i], ow[2 * i + 1], pack2(o[2 * i], o[2 * i + 1]));
      *lw = asu(tr_reduce8(acc, lane));
      BARRIER();
      if (w == 0) {
        const v4u t0 = lr[0], t1 = lr[16], t2 = lr[32], t3 = lr[48];
        v4u r;
        UNR for (int e = 0; e < 4; ++e) r[e] = canon((asf(t0[e]) + asf(t1[e])) + (asf(t2[e]) + asf(t3[e])));
        st4(ws, bvo, bso, r);
      }
    }
    return;
  }
  {
    if (!mlp && (!lds_wait_flag(LV + L_QF) || !lds_wait_flag(LV + L_TF))) return;
    const int cg = lane & 15, rq = lane >> 4;
    v4u wf[40];
    if (mlp && wave < 4) {
      const GAS v4u* gp = (const GAS v4u*)((cg < 8 ? p.wg : p.wu) + (size_t)(160 * w + 8 * rq) * F + 64 * j + 8 * (cg & 7));
      UNR for (int i = 0; i < 40; ++i) { if (i >= GW) vm_wait<GW - 1>(); v4u t_ = __builtin_nontemporal_load(gp + (32 * (i >> 3) + (i & 7)) * (F / 8)); SCHED(); wf[i] = t_; }
      UNR for (int c = 0; c < 5; ++c) to_frags(wf + 8 * c);
      if (!lds_wait_flag(LV + L_GF)) return;
      T = __builtin_amdgcn_readfirstlane(L[L_T]);
    } else {
      const int grp = wave >> 2, blk = grp ? h : 32 + xi;
      u32 sp[10];
      const u32 cw0 = ld1<0>(rc, lane * 4, w * 320), cw1 = ld1<0>(rc, (lane & 15) * 4 + 256, w * 320);
      u32 E = 0;
      if (wave == 4) E = ld1<AUX_SC1>(ws, 0, WS_EPOCH);
      UNR for (int i = 0; i < 20; ++i) { if (i >= MW) vm_wait<MW - 1>(); wf[20 + i] = MOD_LD(i, 2 * w, 2560 + 128 * blk); SCHED(); }
      if (wave == 4) { L[L_T] = MKTAG(E); LDS_WAIT(); LV[L_GF] = 1u; }
      LAS u32* si = L + L_SPILL + wave * 80;
      si[lane] = pack2(silu(lo_f(cw0)), silu(hi_f(cw0)));
      si[64 + (lane & 15)] = pack2(silu(lo_f(cw1)), silu(hi_f(cw1)));
      LDS_WAIT();
      UNR for (int i = 0; i < 10; ++i) sp[i] = si[8 * i + r8];
      asm volatile("" :: "v"(cpf));
      float acc[8] = ZERO8;
      UNR for (int i = 0; i < 10; ++i) { pair_rows(wf[20 + 2 * i], wf[21 + 2 * i]); mac8(acc, wf[20 + 2 * i], wf[21 + 2 * i], sp[i]); }
      L[L_RED + wave * 64 + 8 * c8 + r8] = asu(tr_reduce8(acc, lane));
      if (!lds_wait_flag(LV + L_GF)) return;
      T = __builtin_amdgcn_readfirstlane(L[L_T]);
      LDS_WAIT();
      u32 arrived = 0;
      if (lane == 0) arrived = __hip_atomic_fetch_add(L + L_KF + grp, 1u, __ATOMIC_RELAXED, __HIP_MEMORY_SCOPE_WORKGROUP);
      if (__builtin_amdgcn_readfirstlane(arrived) == 3u) {
        CFENCE();
        const LAS u32* a = L + L_RED + grp * 256 + lane;
        const u32 v = asu(rb((asf(a[0]) + asf(a[64])) + (asf(a[128]) + asf(a[192])))), vn = dppu<XOR1>(v);
        if (!(lane & 1)) st4(ws, lane * 8, WS_MG + blk * 512, v4u{v, T, vn, T});
      }
      if (!mlp) {
        const rsrc_t rcode = mk((const void*)(pc0 & ~127ull), code_bytes);
        u32 acc2 = 0;
        UNR for (int i = 0; i < 5; ++i) acc2 |= ld4<0>(rcode, lane * 16, (wave + 8 * i) * 1024).x;
        asm volatile("" :: "v"(acc2));
        return;
      }
      u32 ex[16];
      const rsrc_t rd = mk(p.wd, F * D * 2);
      UNR for (int i = 0; i < 16; ++i) { vm_wait<2 * MW - 2>(); wf[i] = ld4<AUX_NT>(rd, rq * 16 * 1280 + cg * 16, (64 * j + i) * 1280 + 256 * w); ex[i] = ld1<AUX_NT>(rd, rq * 16 * 1280 + cg * 4, (64 * j + i) * 1280 + 1024 + 64 * w); SCHED(); }
      to_frags(wf); to_frags(wf + 8);
      UNR for (int c = 0; c < 2; ++c) UNR for (int k = 0; k < 4; ++k) { wf[16 + 2 * c][k] = perm_lo(ex[8 * c + 2 * k], ex[8 * c + 2 * k + 1]); wf[17 + 2 * c][k] = perm_hi(ex[8 * c + 2 * k], ex[8 * c + 2 * k + 1]); }
      UNR for (int i = 16; i < 20; ++i) KEEPV(wf[i]);
    }
    const bool two = (wave & 6) == 4;
    const int cc = 4 * cg + rq, col0 = 64 * wave + cc, col1 = 512 + 64 * (wave & 1) + cc, colf = (j < 20 ? 32 * j : 0) + 4 * c8 + rq;
    float xv0 = lo_f(p.x[col0]), xv1 = lo_f(p.x[col1]);
    float ga0, ga1, sb0, sb1, cb0, cb1, gbv;
    u32 near;
    {
      v2u g[7]; v4u pr;
      POLL(16, UNR for (int k = 0; k < 3; ++k) { g[2 * k] = ld2<AUX_SC1>(ws, col0 * 8, WS_MG + 640 * 8 * k); g[2 * k + 1] = ld2<AUX_SC1>(ws, col1 * 8, WS_MG + 640 * 8 * k); }
           g[6] = ld2<AUX_SC1>(ws, colf * 8, WS_MG + 1920 * 8); pr = ld4<AUX_SC1>(ws, lane < 32 ? WS_CL + (lane < 24 ? lane : 0) * 16 : WS_MG + (lane - 32) * 512, 0),
           UNR for (int i = 0; i < 7; ++i) TAG2(g[i]); TAG4(pr));
      near = near_mask(pr);
      ga0 = asf(g[0].x); ga1 = asf(g[1].x); sb0 = asf(g[2].x); sb1 = asf(g[3].x); cb0 = rb(1.f + asf(g[4].x)); cb1 = rb(1.f + asf(g[5].x)); gbv = asf(g[6].x);
    }
    const int mypar = near >> (b & 7) & 1, jm = 4 * w + (cg >> 2);
    int dvo = rq < 2 ? DP_AT(jm, near >> (jm / 3) & 1) + (cg & 3) * 32 + rq * 16 + j * 128 : OOB;
    int evo = rq == 0 ? DP_AT(16 + w, near >> ((16 + w) / 3) & 1) + cg * 8 + j * 128 : OOB;
    int bso0 = BP_AT(wave, mypar), bso1 = two ? BP_AT(4 + wave, mypar) : OOB, fso = DP_AT(j, mypar), ovo = (lane & 8) ? OOB : colf * 2, l16 = lane * 16;
    LAS u16* lx0 = (LAS u16*)(L + L_X2) + col0;
    LAS u16* lx1 = two ? (LAS u16*)(L + L_X2) + col1 : (LAS u16*)(L + L_SPILL + 1024) + lane;
    LAS u32* lst = L + L_ST + 4 * wave + (lane >> 5);
    const LAS v4u* lsr = (const LAS v4u*)(L + L_ST) + c8;
    const LAS v4u* laf = (const LAS v4u*)(L + L_H2 + 80 * w + 4 * rq);
    LAS u32* lgu = L + L_GU + (wave < 4 ? w * 128 + 8 * cg : lane);
    float cnt = (c8 & 6) == 4 ? 128.f : 64.f, rcnt = (c8 & 6) == 4 ? 1.f / 128 : 1.f / 64;
    KEEPV(dvo); KEEPV(evo); KEEPS(bso0); KEEPS(bso1); KEEPS(fso); KEEPV(ovo); KEEPV(l16); KEEPV(lx0); KEEPV(lx1); KEEPV(lst); KEEPV(lsr); KEEPV(laf); KEEPV(lgu); KEEPV(cnt); KEEPV(rcnt);
    KEEPV(xv0); KEEPV(xv1); KEEPV(cb0); KEEPV(cb1);
    const rsrc_t rout = mk(p.out, D * 2);
    if (wave == 7) {
      const int cvo = (lane < 7 ? lane : 0) * OP_S + 764;
      u32 g = ld1<AUX_SC1>(ws, cvo, OP_AT(h, mypar));
      const bool early = !all_zero(g == SENT);
      if (early) SPOLL(4, g = ld1<AUX_SC1>(ws, cvo, OP_AT(h, mypar)), bad = g == SENT);
      L[L_PF] = early;
    }
    BARRIER();
    if (__builtin_amdgcn_readfirstlane(L[L_PF])) __builtin_amdgcn_s_sleep(BDELAY);
    float x20, x21;
    {
      v4u g[8]; float a0, a1;
      GATHER(BSLEEP, UNR for (int i = 0; i < 8; ++i) g[i] = ld4<AUX_SC1>(ws, l16 + (i & 3) * 1024, i < 4 ? bso0 : bso1),
             a0 = fold4((asf4(g[0]) + asf4(g[1])) + (asf4(g[2]) + asf4(g[3]))); a1 = fold4((asf4(g[4]) + asf4(g[5])) + (asf4(g[6]) + asf4(g[7]))), any_nan(a0 + a1),
             bad = umax(umax(umax4(umax4(0u, g[0]), g[1]), umax4(umax4(0u, g[2]), g[3])), umax(umax4(umax4(0u, g[4]), g[5]), umax4(umax4(0u, g[6]), g[7]))) == SENT);
      x20 = rb(xv0 + rb(ga0 * rb(a0))); x21 = rb(xv1 + rb(ga1 * rb(a1)));
      const float pv = asf(__builtin_amdgcn_readfirstlane(asu(x20)));
      const float d0 = x20 - pv, d1 = two ? x21 - pv : 0.f;
      float t = tr32(d0 + d1, fmaf(d0, d0, d1 * d1));
      t = sum16(tr16(t, t));
      lst[0] = asu(t); lst[2] = asu(pv);
      *lx0 = (u16)(asu(x20) >> 16);
      *lx1 = (u16)(asu(x21) >> 16);
      BARRIER();
    }
    {
      const v4u st = *lsr;
      const float mk_ = fmaf(asf(st.x), rcnt, asf(st.z)), m2k = fmaf(-asf(st.x) * rcnt, asf(st.x), asf(st.y));
      const float mean = sum8(mk_ * cnt) * (1.f / D), e = mk_ - mean;
      const float var = rb(sum8(fmaf(e * cnt, e, m2k)) * (1.f / D));
      const float rden = frcp(rb(fsqrt(rb(var + 1e-5f)))), mb = rb(mean);
      u32 hp = pack2(rb(rb(rb(x20 - mb) * rden) * cb0) + sb0, rb(rb(rb(x21 - mb) * rden) * cb1) + sb1);
      KEEPV(hp);
      lx0[2 * (L_H2 - L_X2)] = (u16)hp;
      lx1[2 * (L_H2 - L_X2)] = (u16)(hp >> 16);
      BARRIER();
    }
    if (wave < 4) {
      v4u af[5];
      UNR for (int c = 0; c < 5; ++c) af[c] = laf[4 * c];
      SCHED();
      f32x4 acc[8];
      UNR for (int e = 0; e < 8; ++e) acc[e] = f32x4{0.f, 0.f, 0.f, 0.f};
      UNR for (int c = 0; c < 5; ++c) UNR for (int e = 0; e < 8; ++e) acc[e] = mfma(af[c], wf[8 * c + e], acc[e]);
      volatile LAS u32* q = lgu;
      UNR for (int e = 0; e < 8; ++e) q[e] = asu(acc[e][0]);
    }
    BARRIER();
    if (wave >= 4) {
      const LAS u32* a = lgu;
      const float gg = rb((asf(a[0]) + asf(a[128])) + (asf(a[256]) + asf(a[384]))), uu = rb((asf(a[64]) + asf(a[192])) + (asf(a[320]) + asf(a[448])));
      const float hh = gg * uu;
      const u32 hq = pack2(hh, dpp<XOR1>(hh));
      const v4u a0 = {dppu<0x150>(hq), dppu<0x152>(hq), dppu<0x154>(hq), dppu<0x156>(hq)}, a1 = {dppu<0x158>(hq), dppu<0x15A>(hq), dppu<0x15C>(hq), dppu<0x15E>(hq)};
      f32x4 acc[10];
      UNR for (int e = 0; e < 10; ++e) acc[e] = mfma(a0, wf[e < 8 ? e : 8 + e], f32x4{0.f, 0.f, 0.f, 0.f});
      UNR for (int e = 9; e >= 0; --e) acc[e] = mfma(a1, wf[e < 8 ? 8 + e : 10 + e], acc[e]);
      st2(ws, evo, 0, v2u{canon(acc[8][0]), canon(acc[9][0])});
      v4u r;
      UNR for (int e = 0; e < 4; ++e) r[e] = canon(rq == 0 ? acc[e][0] : acc[4 + e][0]);
      st4(ws, dvo, 0, r);
    } else if (j < 20 && wave == 3) {
      float x2 = lo_f(((const LAS u16*)(L + L_X2))[colf]);
      KEEPV(x2);
      __builtin_amdgcn_s_sleep(DDELAY);
      v4u g[3]; float r;
      GATHER(DSLEEP, UNR for (int i = 0; i < 3; ++i) g[i] = ld4<AUX_SC1>(ws, l16 + i * 1024, fso),
             r = fold4((asf4(g[0]) + asf4(g[1])) + asf4(g[2])); r += dpp<ROR8>(r), any_nan(r),
             bad = umax4(umax4(umax4(0u, g[0]), g[1]), g[2]) == SENT);
      const float o = x2 + rb(gbv * rb(r));
      __builtin_amdgcn_raw_buffer_store_b16((u16)pack2(o, o), rout, ovo, 0, 0);
      asm volatile(".Lfb_last:");
    } else if (j == 0 && wave == 0 && lane == 0) {
      st1(ws, 0, WS_EPOCH, T);
    }
  }
}

extern "C" void kernel_launch(void* const* d_in, const int* in_sizes, int n_in, void* d_out, int out_size, void* d_ws, size_t ws_size, hipStream_t stream) {
  Params p;
  p.x = (const u16*)d_in[0]; p.K = (const u16*)d_in[1]; p.V = (const u16*)d_in[2]; p.wq = (const u16*)d_in[3]; p.wo = (const u16*)d_in[4];
  p.wg = (const u16*)d_in[6]; p.wu = (const u16*)d_in[7]; p.wd = (const u16*)d_in[8]; p.c = (const u16*)d_in[9]; p.wmod = (const u16*)d_in[10];
  p.out = (u16*)d_out; p.ws = (unsigned char*)d_ws;
  hipLaunchKernelGGL(fused_block, dim3(NWG), dim3(NTHR), 0, stream, p);
}
```
